```python
import torch

def _softmax(x, axis=-1):
    return torch.softmax(x, dim=axis)
def _gelu(x):
    return torch.nn.functional.gelu(x, approximate="tanh")
def _silu(x):
    return torch.nn.functional.silu(x)
def _sigmoid(x):
    return torch.sigmoid(x)
def _layernorm(x, axis=-1, eps=1e-5):
    m = x.mean(dim=axis, keepdim=True)
    v = x.var(dim=axis, keepdim=True, unbiased=False)
    return (x - m) / torch.sqrt(v + eps)
def _scatter_add(tgt, idx, src, axis=0):
    return tgt.clone().index_add_(axis, idx, src)
def _segment_sum(src, seg, n):
    out = torch.zeros((n,) + src.shape[1:], dtype=src.dtype)
    return out.index_add_(0, seg, src)
def _pad(x, pw):
    flat = tuple(v for pair in reversed(pw) for v in pair)
    return torch.nn.functional.pad(x, flat)

kernel_name = 'interleave_layers__sliding_local_dilated__ssm_conv_h3__prompt'

def setup_inputs():

    _w = (lambda a: (lambda t: t.to(torch.bfloat16) if t.is_floating_point() else t)(torch.as_tensor(a)))
    def _A_setup():
        B, S, D = 1, 2048, 576
        return {
            "x": _w((lambda *s: torch.randn(*s, dtype=torch.bfloat16) * 0.05)(B, S, D)),
            "wqkv": _w((lambda *s: torch.randn(*s, dtype=torch.bfloat16) * 0.05)(D, 3 * D)),
            "wo": _w((lambda *s: torch.randn(*s, dtype=torch.bfloat16) * 0.05)(D, D)),
        }
    _A_in = _A_setup()

    def _B_setup():
        B, S, D = 1, 2048, 576
        ks = 4
        return {
            "q": _w((lambda *s: torch.randn(*s, dtype=torch.bfloat16) * 0.05)(B, S, D)),
            "k": _w((lambda *s: torch.randn(*s, dtype=torch.bfloat16) * 0.4)(B, S, D)),
            "v": _w((lambda *s: torch.randn(*s, dtype=torch.bfloat16) * 0.4)(B, S, D)),
            "shift_k": _w((lambda *s: torch.randn(*s, dtype=torch.bfloat16) * 0.4)(D, ks) * 0.5),
            "ssm_k": _w((lambda *s: torch.randn(*s, dtype=torch.bfloat16) * 0.8)(D, S) * (1.0 / S ** 0.5)),
            "ssm_D": _w((lambda *s: torch.randn(*s, dtype=torch.bfloat16) * 0.4)(D)),
        }
    _B_in = _B_setup()

    return {
        "A_x": _A_in["x"],
        "A_wqkv": _A_in["wqkv"],
        "A_wo": _A_in["wo"],
        "B_q": _B_in["q"],
        "B_k": _B_in["k"],
        "B_v": _B_in["v"],
        "B_shift_k": _B_in["shift_k"],
        "B_ssm_k": _B_in["ssm_k"],
        "B_ssm_D": _B_in["ssm_D"],
    }

def reference(A_x, A_wqkv, A_wo, B_q, B_k, B_v, B_shift_k, B_ssm_k, B_ssm_D):
    def _A_ref(x, wqkv, wo):
        B, S, D = x.shape
        H = 32; Dh = D // H
        W, dil = 8, 2
        qkv = (lambda x, *d: x.permute(*d))((x @ wqkv).reshape(B, S, 3, H, Dh), 2, 0, 3, 1, 4)
        q, k, v = qkv[0], qkv[1], qkv[2]
        scale = Dh ** -0.5
        pos = torch.arange(S)
        off = torch.arange(W) * dil
        idx = pos[:, None] - off[None, :]
        mask = idx >= 0
        idx_c = torch.where(mask, idx, 0).reshape(-1)
        kg = k[:, :, idx_c, :].reshape(B, H, S, W, Dh)
        vg = v[:, :, idx_c, :].reshape(B, H, S, W, Dh)
        logits = torch.einsum("bhsd,bhswd->bhsw", q, kg) * scale
        logits = torch.where(mask[None, None, :, :], logits, -1e9)
        attn = _softmax(logits, axis=-1)
        o = torch.einsum("bhsw,bhswd->bhsd", attn, vg)
        return (lambda x, *d: x.permute(*d))(o, 0, 2, 1, 3).reshape(B, S, D) @ wo

    def _B_ref(q, k, v, shift_k, ssm_k, ssm_D):
        B, S, D = q.shape
        ks = shift_k.shape[-1]
        k_pad = _pad(k, ((0, 0), (ks - 1, 0), (0, 0)))
        k_shift = torch.zeros_like(k)
        for t in range(ks):
            k_shift = k_shift + k_pad[:, t:t + S, :] * shift_k[:, ks - 1 - t][None, None, :]
        kv = k_shift * v
        L = 2 * S
        kv_pad = _pad(kv, ((0, 0), (0, S), (0, 0)))
        kf_pad = _pad(ssm_k, ((0, 0), (0, S)))
        kv_f = (lambda x, axes=(-2,-1): torch.fft.rfft2(x.float(), dim=axes))(kv_pad[:, None, :, :], axes=(1, 2))
        kf_f = (lambda x, axes=(-2,-1): torch.fft.rfft2(x.float(), dim=axes))(kf_pad[:, None, :], axes=(1, 2))
        kf_f = (lambda x, *d: x.permute(*d))(kf_f, 1, 2, 0)
        y_f = kv_f * kf_f[None, :, :, :]
        y = (lambda x, s, axes=(-2,-1): torch.fft.irfft2(x, s=s, dim=axes).to(torch.bfloat16))(y_f, (1, L), axes=(1, 2))[:, 0, :S, :]
        y = y + kv * ssm_D[None, None, :]
        return q * y

    x = A_x
    x = x + _A_ref(_layernorm(x, axis=-1), A_wqkv, A_wo)
    x = x + _B_ref(_layernorm(x, axis=-1), B_k, B_v, B_shift_k, B_ssm_k, B_ssm_D)
    x = x + _A_ref(_layernorm(x, axis=-1), A_wqkv, A_wo)
    x = x + _B_ref(_layernorm(x, axis=-1), B_k, B_v, B_shift_k, B_ssm_k, B_ssm_D)
    return x
```

```cpp
#include <hip/hip_runtime.h>
#include <hip/hip_bf16.h>
#include <stdint.h>
typedef unsigned short u16;
constexpr int S=2048, D=576, H=32, DH=18;
__device__ __forceinline__ float bf2f(u16 x){ return __uint_as_float(((unsigned)x)<<16); }
__device__ __forceinline__ u16 f2bf(float f){ unsigned u=__float_as_uint(f); return (u16)((u + 0x7FFF + ((u>>16)&1))>>16); }
__device__ __forceinline__ float rb(float f){ return bf2f(f2bf(f)); }

__global__ void k_ln(const u16* x, u16* h){
  int r=blockIdx.x, l=threadIdx.x;
  float v[9]; float s=0;
  for(int i=0;i<9;i++){ v[i]=bf2f(x[r*D+l+64*i]); s+=v[i]; }
  for(int o=32;o;o>>=1) s+=__shfl_xor(s,o);
  float m=s/D; float q=0;
  for(int i=0;i<9;i++){ float d=v[i]-m; q+=d*d; }
  for(int o=32;o;o>>=1) q+=__shfl_xor(q,o);
  float var=rb(q/D); float mb=rb(m);
  float sd=rb(sqrtf(rb(var+1e-5f)));
  for(int i=0;i<9;i++) h[r*D+l+64*i]=f2bf(rb(v[i]-mb)/sd);
}
__global__ void k_gemm(const u16* A, const u16* W, u16* C, int N, const u16* res){
  int n=blockIdx.x*64+threadIdx.x, r=blockIdx.y;
  float acc=0;
  for(int k=0;k<D;k++) acc+=bf2f(A[r*D+k])*bf2f(W[k*N+n]);
  if(res) C[r*N+n]=f2bf(bf2f(res[r*N+n])+rb(acc)); else C[r*N+n]=f2bf(acc);
}
__global__ void k_attn(const u16* qkv, u16* o){
  int r=blockIdx.x, h=threadIdx.x; if(h>=H) return;
  float scale=(float)(1.0/sqrt(18.0));
  float lg[8]; float mx=-1e30f;
  for(int w=0;w<8;w++){
    int idx=r-2*w; float l;
    if(idx>=0){ float a=0; for(int d=0;d<DH;d++) a+=bf2f(qkv[r*3*D+h*DH+d])*bf2f(qkv[idx*3*D+D+h*DH+d]); l=rb(rb(a)*scale);} else l=rb(-1e9f);
    lg[w]=l; mx=fmaxf(mx,l);
  }
  float sum=0; for(int w=0;w<8;w++){ lg[w]=expf(lg[w]-mx); sum+=lg[w]; }
  for(int w=0;w<8;w++) lg[w]=rb(lg[w]/sum);
  for(int d=0;d<DH;d++){ float a=0; for(int w=0;w<8;w++){ int idx=r-2*w; if(idx<0) idx=0; a+=lg[w]*bf2f(qkv[idx*3*D+2*D+h*DH+d]); } o[r*D+h*DH+d]=f2bf(a); }
}
__global__ void k_kv(const u16* k, const u16* v, const u16* sk, u16* kv){
  int d=blockIdx.x*64+threadIdx.x, s=blockIdx.y;
  float a=0;
  for(int t=0;t<4;t++){ int i=s+t-3; float kk= i>=0? bf2f(k[i*D+d]) : 0.f; a=rb(a+rb(kk*bf2f(sk[d*4+3-t]))); }
  kv[s*D+d]=f2bf(a*bf2f(v[s*D+d]));
}
__global__ void k_conv(const u16* kv, const u16* hk, const u16* Dv, u16* y){
  int d=blockIdx.x*64+threadIdx.x, s=blockIdx.y;
  float a=0;
  for(int t=0;t<=s;t++) a+=bf2f(kv[t*D+d])*bf2f(hk[d*S+s-t]);
  y[s*D+d]=f2bf(rb(a)+rb(bf2f(kv[s*D+d])*bf2f(Dv[d])));
}
__global__ void k_gate(const u16* x, const u16* h, const u16* y, u16* out){
  int i=blockIdx.x*256+threadIdx.x;
  out[i]=f2bf(bf2f(x[i])+rb(bf2f(h[i])*bf2f(y[i])));
}
extern "C" void kernel_launch(void* const* d_in, const int* in_sizes, int n_in, void* d_out, int out_size, void* d_ws, size_t ws_size, hipStream_t stream){
  const u16* x=(const u16*)d_in[0]; const u16* wqkv=(const u16*)d_in[1]; const u16* wo=(const u16*)d_in[2];
  const u16* k=(const u16*)d_in[4]; const u16* v=(const u16*)d_in[5]; const u16* sk=(const u16*)d_in[6]; const u16* hk=(const u16*)d_in[7]; const u16* Dv=(const u16*)d_in[8];
  u16* ws=(u16*)d_ws; size_t SD=(size_t)S*D;
  u16* h=ws; u16* qkv=ws+SD; u16* o=ws+4*SD; u16* x1=ws+5*SD; u16* x2=ws+6*SD; u16* kv=ws+7*SD; u16* y=ws+8*SD; u16* x3=ws+9*SD;
  u16* out=(u16*)d_out;
  k_kv<<<dim3(9,S),64,0,stream>>>(k,v,sk,kv);
  k_conv<<<dim3(9,S),64,0,stream>>>(kv,hk,Dv,y);
  const u16* xin=x;
  for(int L=0;L<2;L++){
    u16* xa = L==0? x1 : x3; u16* xb = L==0? x2 : out;
    k_ln<<<S,64,0,stream>>>(xin,h);
    k_gemm<<<dim3(27,S),64,0,stream>>>(h,wqkv,qkv,3*D,nullptr);
    k_attn<<<S,64,0,stream>>>(qkv,o);
    k_gemm<<<dim3(9,S),64,0,stream>>>(o,wo,xa,D,xin);
    k_ln<<<S,64,0,stream>>>(xa,h);
    k_gate<<<S*D/256,256,0,stream>>>(xa,h,y,xb);
    xin=xb;
  }
}
```

```cpp
#include <hip/hip_runtime.h>
#include <stdint.h>
#include <utility>

typedef unsigned int u32;
typedef unsigned long long u64;
typedef __attribute__((ext_vector_type(4))) u32 u32x4;
typedef __attribute__((ext_vector_type(2))) u32 u32x2;
typedef __attribute__((ext_vector_type(4))) float f32x4;
typedef __attribute__((ext_vector_type(2))) float f32x2;
typedef __attribute__((ext_vector_type(8))) __bf16 bf16x8;
typedef __attribute__((ext_vector_type(2))) __bf16 bf16x2;
typedef __attribute__((ext_vector_type(4))) short s16x4;
typedef __amdgpu_buffer_rsrc_t rsrc_t;
#define AS3 __attribute__((address_space(3)))
#define DEV __device__ __forceinline__
#define ANY(x) (__builtin_amdgcn_ballot_w64(x) != 0)
#define U2F(x) __builtin_bit_cast(float, (u32)(x))
#define F2U(x) __builtin_bit_cast(u32, (float)(x))
DEV u32 mul24(u32 a, u32 b) { return (a & 0xFFFFFFu) * (b & 0xFFFFFFu); }

constexpr u32 WS_E = 0;
constexpr u32 WS_FLAG = 4096;
constexpr u32 WS_PF = 8192;
constexpr u32 WS_XS = 16384;
constexpr u32 WS_ST = 65536;
constexpr u32 WS_KVT = 1u << 20;
constexpr u32 WS_SET = 4u << 20;
constexpr u32 GRP_B = 73728;
constexpr u32 REG_B = 4 * GRP_B;
constexpr u32 SET_B = 3 * REG_B;
constexpr u32 YC_B = 6144, YM_B = 96 * YC_B;
constexpr u32 WS_Y = WS_SET + 8 * SET_B;
constexpr u32 WS_WO = WS_Y + 8 * YM_B;
constexpr u32 WS_WP = WS_WO + 8 * 82944;
constexpr u32 WS_USED = WS_WP + 8 * 110592;
constexpr u32 WS_DBG = 18u << 20;
constexpr u32 SENT = 0xFFFFFFFFu;
constexpr u32 SLOT_B = 37888, WSL_B = 27648;
constexpr int SLOT(int s) { return (s + 2) % 3; }
constexpr u32 L_KV = 3 * SLOT_B;
constexpr u32 L_QKV = 0, QP = 456;
constexpr u32 L_WO = 37888;
constexpr u32 L_CONV = 120832;
constexpr u32 KV_B = 4640, H_B = 8288, L_H2 = L_CONV + 3 * KV_B, L_H0 = L_H2 + H_B;
constexpr u32 CH_O = 0, CH_E = 4160;
constexpr u32 L_WS = L_H2 + 64;
constexpr u32 L_PS = L_H0 + 64;
constexpr u32 L_MISC = 159744;
constexpr u32 L_STAT = L_MISC + 64;
constexpr u32 L_CS = L_MISC + 1024;
constexpr u32 L_SYNC = L_MISC + 2048;
constexpr u32 L_TAG = L_SYNC + 92;
constexpr u32 L_ONE = L_MISC + 2304;
constexpr u32 L_RED = L_CONV + 2 * KV_B;
constexpr u32 L_Y = L_H0, YP = 160;
constexpr int SPIN_MAX = 1 << 16;
constexpr u32 OOB = 0x7FFFFFF0u;
constexpr int KS[18] = {2, 4, 6, 8, 11, 13, 15, 0, 1, 3, 5, 7, 9, 10, 12, 14, 16, 17};
constexpr u32 xk_addr(int p, int t) { return t == 4 ? L_CONV + p * 1024 : (p < 7 ? L_CONV + p * 4096 : p < 16 ? (p - 7) * 4096 : L_CONV + (p - 9) * 4096) + t * 1024; }
constexpr int g2_t(int n) { return n < 64 ? n & 3 : n < 88 && (n - 64) % 6 < 2 ? (n - 64) / 6 : 4; }
constexpr int g2_p(int n) { return n < 64 ? n >> 2 : n >= 88 ? n - 72 : (n - 64) % 6 < 2 ? 16 + (n - 64) % 6 : (n - 64) / 6 * 4 + (n - 64) % 6 - 2; }

DEV f32x4 mfma(u32x4 a, u32x4 b, f32x4 c) {
  return __builtin_amdgcn_mfma_f32_16x16x32_bf16(__builtin_bit_cast(bf16x8, a), __builtin_bit_cast(bf16x8, b), c, 0, 0, 0);
}
DEV void mfa(f32x4& c, u32x4 a, u32x4 b) { asm volatile("v_mfma_f32_16x16x32_bf16 %0, %1, %2, %0" : "+v"(c) : "v"(a), "v"(b)); }
DEV u32 pk(float lo, float hi) { bf16x2 r; r.x = (__bf16)lo; r.y = (__bf16)hi; return __builtin_bit_cast(u32, r); }
DEV float blo(u32 v) { return U2F(v << 16); }
DEV float bhi(u32 v) { return U2F(v & 0xFFFF0000u); }
DEV float rb(float x) { return blo(pk(x, x)); }
DEV u32 nos(u32 v) { return v < 0xFFFFFFFEu ? v : 0xFFFFFFFEu; }
DEV u32x2 trd(AS3 char* p) { return __builtin_bit_cast(u32x2, __builtin_amdgcn_ds_read_tr16_b64_v4i16((AS3 s16x4*)p)); }
DEV u32x4 tr2(AS3 char* p0, AS3 char* p1) { u32x2 a = trd(p0), b = trd(p1); return u32x4{a.x, a.y, b.x, b.y}; }
DEV float xmax(float x) {
  auto a = __builtin_amdgcn_permlane32_swap(F2U(x), F2U(x), false, false);
  x = __builtin_fmaxf(U2F(a[0]), U2F(a[1]));
  auto b = __builtin_amdgcn_permlane16_swap(F2U(x), F2U(x), false, false);
  return __builtin_fmaxf(U2F(b[0]), U2F(b[1]));
}
template <int O0, int O1> DEV u32x4 lds2(u32 a) {
  u32x4 v; asm volatile("ds_read2_b64 %0, %1 offset0:%2 offset1:%3" : "=v"(v) : "v"(a), "n"(O0), "n"(O1) : "memory"); return v;
}
template <int O> DEV void ldhi(u32& z, u32 a) { asm volatile("ds_read_u16_d16_hi %0, %1 offset:%2" : "+v"(z) : "v"(a), "n"(O) : "memory"); }
template <int N> DEV void lgw(u32x4& a, u32x4& b, u32x4& c) { asm volatile("s_waitcnt lgkmcnt(%3)" : "+v"(a), "+v"(b), "+v"(c) : "n"(N)); }
DEV float fmx(float a, float b) { return __builtin_elementwise_maximum(a, b); }
DEV void sw32(float& a, float& b) { auto t = __builtin_amdgcn_permlane32_swap(F2U(a), F2U(b), false, false); a = U2F(t[0]); b = U2F(t[1]); }
DEV void sw16(float& a, float& b) { auto t = __builtin_amdgcn_permlane16_swap(F2U(a), F2U(b), false, false); a = U2F(t[0]); b = U2F(t[1]); }
DEV u32 mx3(u32 a, u32 b, u32 c) { u32 t = a > b ? a : b; return t > c ? t : c; }
DEV float xsum(float x) {
  auto a = __builtin_amdgcn_permlane32_swap(F2U(x), F2U(x), false, false);
  x = U2F(a[0]) + U2F(a[1]);
  auto b = __builtin_amdgcn_permlane16_swap(F2U(x), F2U(x), false, false);
  return U2F(b[0]) + U2F(b[1]);
}
DEV void xsum2(float& x, float& y) {
  auto a = __builtin_amdgcn_permlane32_swap(F2U(x), F2U(x), false, false), c = __builtin_amdgcn_permlane32_swap(F2U(y), F2U(y), false, false);
  x = U2F(a[0]) + U2F(a[1]); y = U2F(c[0]) + U2F(c[1]);
  asm("" : "+v"(x), "+v"(y));
  auto b = __builtin_amdgcn_permlane16_swap(F2U(x), F2U(x), false, false), d = __builtin_amdgcn_permlane16_swap(F2U(y), F2U(y), false, false);
  x = U2F(b[0]) + U2F(b[1]); y = U2F(d[0]) + U2F(d[1]);
  asm("" : "+v"(x), "+v"(y));
}
DEV void ln_scalars(float sum, float sq, float& mu, float& rho) {
  float mean = sum * (1.0f / 576.0f);
  float var = __builtin_fmaxf(sq * (1.0f / 576.0f) - mean * mean, 0.0f);
  mu = rb(mean);
  float sd = rb(__builtin_amdgcn_sqrtf(rb(rb(var) + 1e-5f)));
  rho = __builtin_amdgcn_rcpf(sd);
}
DEV void dma16(rsrc_t rs, u32 voff, u32 soff, u32 lds) {
  asm volatile("s_mov_b32 m0, %0\n\ts_nop 3\n\tbuffer_load_dwordx4 %1, %2, %3 offen lds" ::"s"(lds), "v"(voff), "s"(rs), "s"(soff) : "memory");
}
template <int LDS, int OFF> DEV void dmai(rsrc_t rs, u32 voff, u32 soff) {
  asm volatile("s_mov_b32 m0, %0\n\ts_nop 0\n\tbuffer_load_dwordx4 %1, %2, %3 offen offset:%4 lds" ::"n"(LDS - OFF), "v"(voff), "s"(rs), "s"(soff), "n"(OFF) : "memory");
}
template <int LDS, int OFF> DEV void dmae(rsrc_t rs, u32 voff, u32 soff) {
  asm volatile("s_mov_b32 m0, %0\n\ts_mov_b32 exec_lo, 0xFFFFFF00\n\tbuffer_load_dwordx4 %1, %2, %3 offen offset:%4 lds\n\ts_mov_b32 exec_lo, -1" ::"n"(LDS - OFF), "v"(voff), "s"(rs), "s"(soff), "n"(OFF) : "memory");
}
template <int LDS, int OFF> DEV void dmaic(rsrc_t rs, u32 voff, u32 soff) {
  asm volatile("s_mov_b32 m0, %0\n\ts_nop 0\n\tbuffer_load_dwordx4 %1, %2, %3 offen offset:%4 sc1 lds" ::"n"(LDS - OFF), "v"(voff), "s"(rs), "s"(soff), "n"(OFF) : "memory");
}
template <int K> DEV void dmak(rsrc_t rs, u32 voff, u32 soff, u32 lds) {
  if (K == 0) asm volatile("s_mov_b32 m0, %0\n\ts_nop 0\n\tbuffer_load_dwordx4 %1, %2, %3 offen sc1 lds" ::"s"(lds), "v"(voff), "s"(rs), "s"(soff) : "memory");
  else asm volatile("buffer_load_dwordx4 %0, %1, %2 offen offset:%3 sc1 lds" ::"v"(voff), "s"(rs), "s"(soff), "n"(K * 1024) : "memory");
}
template <int LDS, int K, int ADD> DEV void dmaf(rsrc_t rs, u32 voff, u32 sb, u32& so) {
  asm volatile("s_mov_b32 m0, %1\n\ts_add_u32 %0, %4, %5\n\tbuffer_load_dwordx4 %2, %3, %0 offen offset:%6 sc1 lds" : "=&s"(so) : "n"(LDS), "v"(voff), "s"(rs), "s"(sb), "n"(ADD), "n"(K * 1024) : "memory", "scc");
}
template <int LDS, int C> DEV void dma4k(rsrc_t rs, u32 voff, u32 soff) {
  if (C) asm volatile("s_mov_b32 m0, %0\n\ts_nop 0\n\tbuffer_load_dwordx4 %1, %2, %3 offen sc1 lds\n\tbuffer_load_dwordx4 %1, %2, %3 offen offset:1024 sc1 lds\n\t"
               "buffer_load_dwordx4 %1, %2, %3 offen offset:2048 sc1 lds\n\tbuffer_load_dwordx4 %1, %2, %3 offen offset:3072 sc1 lds" ::"n"(LDS), "v"(voff), "s"(rs), "s"(soff) : "memory");
  else asm volatile("s_mov_b32 m0, %0\n\ts_nop 0\n\tbuffer_load_dwordx4 %1, %2, %3 offen lds\n\tbuffer_load_dwordx4 %1, %2, %3 offen offset:1024 lds\n\t"
               "buffer_load_dwordx4 %1, %2, %3 offen offset:2048 lds\n\tbuffer_load_dwordx4 %1, %2, %3 offen offset:3072 lds" ::"n"(LDS), "v"(voff), "s"(rs), "s"(soff) : "memory");
}
template <class F, int... I> DEV void sfor_(F& f, std::integer_sequence<int, I...>) { (f(std::integral_constant<int, I>{}), ...); }
template <int N, class F> DEV void sfor(F f) { sfor_(f, std::make_integer_sequence<int, N>{}); }
#define RFL(x) __builtin_amdgcn_readfirstlane(x)
DEV u32x2 hld64(rsrc_t rs, u32 voff) {
  u32x2 v; asm volatile("s_nop 4\n\tbuffer_load_dwordx2 %0, %1, %2, 0 offen" : "=v"(v) : "v"(voff), "s"(rs) : "memory"); return v;
}
DEV void dma4(rsrc_t rs, u32 voff, u32 soff, u32 lds) {
  asm volatile("s_mov_b32 m0, %0\n\ts_nop 3\n\tbuffer_load_dword %1, %2, %3 offen lds" ::"s"(lds), "v"(voff), "s"(rs), "s"(soff) : "memory");
}
DEV void dma16c(rsrc_t rs, u32 voff, u32 soff, u32 lds) {
  asm volatile("s_mov_b32 m0, %0\n\ts_nop 3\n\tbuffer_load_dwordx4 %1, %2, %3 offen sc1 lds" ::"s"(lds), "v"(voff), "s"(rs), "s"(soff) : "memory");
}
DEV u32x4 hld128(rsrc_t rs, u32 voff) {
  u32x4 v; asm volatile("s_nop 4\n\tbuffer_load_dwordx4 %0, %1, %2, 0 offen" : "=v"(v) : "v"(voff), "s"(rs) : "memory"); return v;
}
DEV u32x4 hld128c(rsrc_t rs, u32 voff) {
  u32x4 v; asm volatile("s_nop 4\n\tbuffer_load_dwordx4 %0, %1, %2, 0 offen sc1" : "=v"(v) : "v"(voff), "s"(rs) : "memory"); return v;
}
template <int OFF> DEV u32x4 hl128(rsrc_t rs, u32 voff, u32 soff) {
  u32x4 v; asm volatile("buffer_load_dwordx4 %0, %1, %2, %3 offen offset:%4 sc1" : "=v"(v) : "v"(voff), "s"(rs), "s"(soff), "n"(OFF) : "memory"); return v;
}
template <int OFF> DEV u32 hl32(rsrc_t rs, u32 voff, u32 soff) {
  u32 v; asm volatile("buffer_load_dword %0, %1, %2, %3 offen offset:%4 sc1" : "=v"(v) : "v"(voff), "s"(rs), "s"(soff), "n"(OFF) : "memory"); return v;
}
DEV u32x2 hl64(rsrc_t rs, u32 voff, u32 soff) {
  u32x2 v; asm volatile("buffer_load_dwordx2 %0, %1, %2, %3 offen sc1" : "=v"(v) : "v"(voff), "s"(rs), "s"(soff) : "memory"); return v;
}
DEV u32 hld32c(rsrc_t rs, u32 voff) {
  u32 v; asm volatile("s_nop 4\n\tbuffer_load_dword %0, %1, %2, 0 offen sc1" : "=v"(v) : "v"(voff), "s"(rs) : "memory"); return v;
}
DEV u32 hld16(rsrc_t rs, u32 voff) {
  u32 v; asm volatile("s_nop 4\n\tbuffer_load_ushort %0, %1, %2, 0 offen" : "=v"(v) : "v"(voff), "s"(rs) : "memory"); return v;
}
#define LD128C(rs, v, s) __builtin_amdgcn_raw_buffer_load_b128(rs, v, s, 16)
#define ST128C(d, rs, v) __builtin_amdgcn_raw_buffer_store_b128(d, rs, v, 0, 16)
#define ST128S(d, v, s) __builtin_amdgcn_raw_buffer_store_b128(d, rws, v, s, 16)
#define ST64C(d, rs, v) __builtin_amdgcn_raw_buffer_store_b64(d, rs, v, 0, 16)
#define ST32C(d, rs, v) __builtin_amdgcn_raw_buffer_store_b32(d, rs, v, 0, 16)
#define VMW(n) asm volatile("s_waitcnt vmcnt(" #n ")" ::: "memory")
template <int N> DEV void vmwn() { static_assert(N >= 0 && N < 64, "vmcnt"); asm volatile("s_waitcnt vmcnt(%0)" ::"n"(N) : "memory"); }
#define VMWN(n) vmwn<(n)>()
#define VM0() __builtin_amdgcn_s_waitcnt(0x0F70)
#define BAR() asm volatile("s_waitcnt lgkmcnt(0)\n\ts_barrier" ::: "memory")
#define SB() __builtin_amdgcn_sched_barrier(0)
#define PIN(x) asm volatile("" : "+v"(x))
#define PINS(x) asm volatile("" : "+s"(x))
#define LM(m) __builtin_amdgcn_inverse_ballot_w64(m)
constexpr u64 lmask(int lo, int hi) { u64 m = 0; for (int l = 0; l < 64; l++) { int d = (l & 15) - 4 * (l >> 4) - 8; if (d >= lo && d < hi) m |= 1ull << l; } return m; }
DEV bool hasS(u32x4 v) { u32 a = v.x > v.y ? v.x : v.y, b = v.z > v.w ? v.z : v.w; return (a > b ? a : b) == SENT; }
DEV bool has2(u32x4 a, u32x4 b) { u32 t = mx3(a.x, a.z, b.x); return (t > b.z ? t : b.z) == SENT; }
DEV u32 seg9(u32 e) { return e * 16 + mul24(mul24(e, 7282u) >> 16, 1008u); }

#define TRY(l) TRYN(l, SPIN_MAX)
#define TRYN(l, n) { int spin = 0; u32 lim = (l); _Pragma("unroll 1") do { if (++spin > (n)) lim = ~0u; PINS(lim); asm volatile("" ::: "memory");
#define AGAIN(x) } while (__builtin_expect(ANY((x) > lim), 0)); }
#define TAG() (*(volatile AS3 u32*)(sm + L_TAG))
#define FLAGS 3
#define KPOS 30
#define B0 64
#define GPRIO 0
#define WOS 6
#define SRS 0
#define XRS 0
#define XOF 4
#define XDLY2 12
#define CLS 0
#define CLD 0
#define HPF -1
#define WOB 8
#define ODLY0 2
#define NPRIO 3
#define NCL 24
#define NB8 6
#define CL0 2
#define CPP 2
#define CLN 9
#define FB 6
#define CLPRIO 3
#define YCS 3
#define WO0 1
#define XCL -1
#define WOA 0
#define WOM 2
#define WOD 1
#define NOP 36
#define NBC -1
#define TP0 3
#define TP4 0
#define CP0 0
#define YDL2 1
constexpr int wo_at(int d) { return WOA + d * WOM / WOD; }
#define SDLY0 9
#define CPRE 3
#define YDLY7 24
#define XW 14
#define XDLY 11
#define PD 5
#define NWO 70
#define NB9 72
#define U_POLL 266
#define U_CHK 306
#define PK0 5
#define P_POLL 159
#define PKL 0
#define BSS 64, 101, 138, 175, 222, 259, 296, 333
#define HPS 223, 223, 223, 223
#define KVP 999
#define CLP 999
#define PREP 110
#define CVS 6
#define CVR 6
#define CVW 5
#define CVD 1
#define WN0 8
#define WOL 20
#define N29 8
#define CS0 999
#define EVS 1
#define NR8 62
#define PBY 0
#define WPS 1
#define EPW 0
#define EPD 1
#define CSW 10
#define TPN -1
#define XB 0
#define XDM 0
#define W7Q 2
#define WOT 44
#define NBS 2
enum { E_KB = 1000, E_FLAG, E_PPOLL, E_PCHK, E_UPOLL, E_UCHK, E_PREP, E_H = 1010, E_KV = 1020, E_BAR = 1030, E_VW = 1040, E_CLR = 1050, E_END = 1060, E_RDW = 1100 };
constexpr int BS[8] = {BSS}, HP[4] = {HPS};
struct Seq { int e[480] = {}, n = 0; constexpr void add(int c) { e[n++] = c; } };
constexpr Seq mkseq() {
  Seq q;
  for (int u = 0; u <= 333; u++) {
    for (int c = 0; c < 4; c++) if (HP[(c + 2) % 4] == u) q.add(E_H + (c + 2) % 4);
    if (u == KPOS) q.add(E_KB);
    if (u == KVP) { q.add(E_KV); q.add(E_KV + 1); }
    for (int s = 0; s < 8; s++) if (BS[s] == u) { q.add(E_END + s); q.add(E_BAR + s); if (s == FLAGS) q.add(E_FLAG); }
    if (u >= 9 && u < 36 && u % 3 == 0) q.add(E_VW + u / 3 - 3);
    if (u == PREP) q.add(E_PREP);
    if (u == P_POLL) q.add(E_PPOLL);
    if (u == 195) q.add(E_PCHK);
    if (u == U_POLL) q.add(E_UPOLL);
    if (u == U_CHK) { q.add(E_UCHK); q.add(E_KV + 2); }
    if (u >= CLP && u < CLP + 9) q.add(E_CLR + u - CLP);
    if (u < 333) q.add(u);
    for (int s = 0; s < 7; s++) if (u >= BS[s] + 2 && u < BS[s] + 6) q.add(E_RDW + 4 * s + u - BS[s] - 2);
  }
  return q;
}
constexpr Seq SQ = mkseq();
constexpr int vm_n(int c) { return c < 333 || c == E_FLAG || c == E_PPOLL || (c >= E_CLR && c < E_END) ? 1 : c == E_UCHK ? 2 : c == E_UPOLL || (c >= E_H && c < E_BAR) ? 4 : 0; }
constexpr int since(int c, int i = SQ.n) { int n = 0; while (i > 0 && SQ.e[i - 1] != c) n += vm_n(SQ.e[--i]); return i > 0 || c < 0 ? n : -1; }
constexpr int sq_at(int u) { int i = 0; while (i < SQ.n && SQ.e[i] != u) i++; return i; }
constexpr bool runs(int i) { for (int k = i - 1; k >= 0; k--) { const int c = SQ.e[k]; if (c < 333 || c == E_UCHK || (c >= E_H && c < E_BAR)) return c == SQ.e[i] - 1; } return false; }
constexpr bool sq_ok() { for (int s = 0; s < 8; s++) if (BS[s] > 37 * (s + 2) || BS[s] < 37 * (s + 1)) return false; for (int c = 0; c < 4; c++) if (HP[c] <= BS[1] || HP[c] > 295 || PREP >= HP[c]) return false; return BS[7] == 333 && U_CHK < 333 && U_CHK > BS[FLAGS] && U_CHK > BS[3] && KVP > U_CHK && PREP < HP[0] && PREP < HP[1] && PREP < HP[2] && PREP < CLP && PREP > BS[1]; }
static_assert(sq_ok(), "schedule");
constexpr int TK = KVP > 333 ? 8 : 0, TC = CLP > 333 ? 9 : 0;
constexpr int wo_n(int n) { int c = WN0; while (c < TK + TC + WOT && (c < WN0 + TC ? wo_at(c - WN0) : WOL + (c - WN0 - TC) / WPS) <= n) c++; return c; }
constexpr int c63(int n) { return n < 63 ? n : 63; }
#define OPM0 1, 0, 4, 3, 2
#define OPM1 1, 0, 4, 3, 2
#define MFA 1
#define VMM 1
constexpr int OPM[2][5] = {{OPM0}, {OPM1}};
#define EP0 5
#define G2X 0
constexpr int g2_m(int n) { return n < 64 ? 2 * n : n < 88 ? 128 + (n - 64) / 6 * 8 + ((n - 64) % 6 < 2 ? 2 * ((n - 64) % 6) : 2 + (n - 64) % 6) : 72 + n; }
#define PD3 8
constexpr int g2_r(int n) { int r = n + PD; if (r > 64) { r = 2 * r - 64; if (r > n + PD3) r = n + PD3; } return r < 90 ? r : 90; }
#define HC2 0
#define PR0 28
#define PRS 2
#define W7B 3
#define AD0 54
#define W7P 2
#define HCS 9
#define HCN 3
#define HCW 8
#define STN 3
#define HCO 70
#define HP0 0
#define YPN 2
#define YPS 1
#define YW 0
#define CAN 0
#define CVA 1
#define CSL 0
#define NFL 68
#define FLE 0
#define YSEL 1
#define TS 5
#define PSP 0
#define G2P -1, -1, -1, -1, 23, 33, 43, 52, 59, 72, 80, 86
constexpr int G2N[12] = {G2P};
constexpr int g2_x(int n) { return n >= G2X ? n : n ^ 1; }
constexpr int vmimm(int n) { return 0x0F70 | (n & 15) | (n >> 4) << 14; }
#define SO 0
#define RPS 3
#define ODLY 8
#define OW0 11
#define OW1 11
#define AW1 10
#define A8 1
#define PAD0 0
#define PADN(n) do { if (n) asm volatile("s_branch .Lpad%=\n\t.fill %0,4,0xBF800000\n.Lpad%=:" :: "n"((n) - 1)); } while (0)
#define EPS 7
#define EPN 8
#define APK 0
#define AW0 7
#define APRIO 3
#define CPRIO 2
#define YVM 3
#define YDLY 24
#define SDLY 8
#define OPRIO 1
#define DG5 0
#define STAMP5(n) do { if (DG5) STAMP(n); } while (0)
#define SW0 0
#define STAMP(n)
#define STAMP2(n)

#define STAMP3(n)
#define STAMP4(n)
#define ENTRY()
#define ENTRY2()
extern "C" __global__ __launch_bounds__(512, 2) void fused_kernel(const void*, const void*, const void*, const void*, void*, const void*, const void*, const void*, const void*, void*) {
  __shared__ __attribute__((aligned(16))) char smem_[163840];
  typedef __attribute__((ext_vector_type(16))) u32 u32x16;
  const u32x16 kx = *(const __attribute__((address_space(4))) u32x16*)__builtin_amdgcn_kernarg_segment_ptr();
  const u32x4 ky = ((const __attribute__((address_space(4))) u32x4*)__builtin_amdgcn_kernarg_segment_ptr())[4];
  int tid0 = __builtin_amdgcn_workitem_id_x();
  const int bp_ = __builtin_amdgcn_workgroup_id_x();
  SB();
  PIN(tid0);
  ENTRY();
  AS3 char* sm = (AS3 char*)smem_;
  const u32 smb = (u32)(uintptr_t)sm;
  const int w0 = __builtin_amdgcn_readfirstlane(tid0 >> 6);
  if (w0 < 2) {
    u64 pc; u32 len;
    asm volatile("s_getpc_b64 %0\n.Lfk_pc:\n\ts_mov_b32 %1, .Lfk_end-.Lfk_pc\n\ts_max_u32 %1, %1, .Lfk_en2-.Lfk_pc" : "=s"(pc), "=s"(len) :: "scc");
    dma4(__builtin_amdgcn_make_buffer_rsrc((void*)pc, 0, len, 0x20000), tid0 * 128, (bp_ & 56) * 2048, L_MISC + 3072);
  }
  const int gl_ = ((bp_ & 6) << 2) + (bp_ >> 5), ml_ = ((bp_ & 1) << 2) + ((bp_ >> 3) & 3), b0 = (gl_ >> 2) + 8 * ((gl_ & 3) * 8 + ml_);
#define IDS() \
  int tid = tid0, w = w0, b = b0; asm volatile("" : "+v"(tid), "+s"(w), "+s"(b)); \
  const int lane = tid & 63, xs = b & 7, r = b >> 3, g = xs * 4 + (r >> 3), m = r & 7, cw = w & 3; \
  const int i = lane & 15, q = lane >> 4, r4 = (lane >> 2) & 3, c4 = lane & 3; \
  const bool isO = w < 4; \
  const u32 setb = WS_SET + xs * SET_B, gb = setb + (r >> 3) * GRP_B; \
  const int gp = g > 0 ? g - 1 : 0, ch0 = xs * 72 + 2 * r, ch2 = xs * 72 + 64 + (r >> 2); \
  (void)i; (void)q; (void)r4; (void)c4; (void)isO; (void)gb; (void)gp; (void)ch0; (void)ch2; (void)setb; (void)m; (void)cw;
  auto P = [](u32 lo, u32 hi) { return (void*)(((u64)hi << 32) | lo); };
  if (w0 == 7) {
    __builtin_amdgcn_s_setprio(2);
    const int lane = tid0 & 63;
    u32 xs = b0 & 7, r = b0 >> 3;
    PINS(xs); PINS(r);
    u32 vk = mul24(lane >> 3, 1152u) + (lane & 7) * 16;
    u32 sk = r * 73728 + xs * 128;
    PIN(vk); PINS(sk);
    u32 vh = (lane < 40 || r == 0) ? OOB : vk, ve = (lane < 40 || b0 == 0) ? OOB : vk;
    u32 se = r * 73728 + xs * 9216 + 1024, skh = r ? sk - 9216 : 0, seh = b0 ? se - 9216 : 0, so[8];
    PIN(vh); PIN(ve); PINS(se); PINS(skh); PINS(seh);
#pragma unroll
    for (int c = 0; c < 8; c++) { so[c] = sk + c * 9216; PINS(so[c]); }
    SB();
    const rsrc_t rk = __builtin_amdgcn_make_buffer_rsrc(P(kx[0], kx[1]), 0, 2048 * 1152, 0x20000), rv = __builtin_amdgcn_make_buffer_rsrc(P(kx[2], kx[3]), 0, 2048 * 1152, 0x20000);
    sfor<20>([&](auto C) { constexpr int c = C;
      dmai<L_KV + c * 1040, 0>(c < 11 ? rk : rv, c == 0 ? vh : c == 9 ? ve : vk, c == 0 ? skh : c < 9 ? so[(c - 1) & 7] : c == 9 ? seh : c == 10 || c == 19 ? se : so[(c - 11) & 7]); });
  }
  u32 aR, aO, aC, aX, aF, aS;
  u64 aM[4];
  auto scal = [&]() {
    IDS();
    const u32 hp = w >> 2, tb = gb + cw * 18432;
    aR = RFL(L_QKV + ((cw & 1) * 40 + (cw >> 1) * 16) * QP + hp * 72);
    aO = RFL(tb + (2 * m + hp) * 1024); aC = RFL(tb + 16384 + m * 256 + hp * 8); aX = RFL(tb + REG_B + m * 2304); aF = WS_FLAG + 4 + (b & ~56) * 16; aS = WS_ST + (g * 4 + cw) * 2048 + m * 256;
    const u64 gm = g == 0 && cw < 2 ? ~0ull << 32 : ~0ull;
#pragma unroll
    for (int e = 0; e < 4; e++) { aM[e] = lmask(e, 64) | (lmask(e - 16, e - 8) & gm); PINS(aM[e]); }
    PINS(aR); PINS(aO); PINS(aC); PINS(aX); PINS(aF); PINS(aS);
  };
  void* const px = P(kx[4], kx[5]); void* const pw = P(kx[6], kx[7]); void* const pws = P(kx[8], kx[9]); void* const psk = P(kx[10], kx[11]); void* const ph = P(kx[12], kx[13]);
  void* const pwo = P(kx[14], kx[15]); void* const pD = P(ky[0], ky[1]); void* const pout = P(ky[2], ky[3]);
  scal();
  const rsrc_t rx = __builtin_amdgcn_make_buffer_rsrc((void*)px, 0, 2048 * 1152, 0x20000);
  const rsrc_t rw = __builtin_amdgcn_make_buffer_rsrc((void*)pw, 0, 576 * 3456, 0x20000);
  const rsrc_t rws = __builtin_amdgcn_make_buffer_rsrc(pws, 0, WS_USED, 0x20000);

  u32x4 wf0[18], wf1[18];
  f32x4 acc[10];
  auto tcol = [&](int w, int f) { return f == 0 ? 144 + 32 * w : w < 4 ? 32 * w : w == 4 ? 112 : w < 7 ? 208 + 32 * w : 400; };
  auto epi = [&](f32x4 a, f32x4 cs, f32x2 st, AS3 char* d) {
    float u[4];
#pragma unroll
    for (int e = 0; e < 4; e++) { u[e] = __builtin_fmaf(-st.x, cs[e], a[e]) * st.y; asm("" : "+v"(u[e])); }
    *(AS3 u32x2*)d = u32x2{pk(u[0], u[1]), pk(u[2], u[3])};
  };
  constexpr auto qrow = [](int t) { return ((t & 1) * 40 + 8 + (t >> 1) * 16) * QP; };
  u32 fl, fa, tg;
  u32 pt, k0, qa, k1, lm0, lm1, oa, oc;
  f32x4 mk;
  u64 md[4], q0;
  AS3 char* v0; AS3 char* v1; AS3 char* e0; AS3 char* e1;
  constexpr int NPR = 12;
  auto prep = [&](auto LY, auto K) {
    constexpr int layer = LY, k = K;
    if constexpr (k == 0) { pt = tid0; PIN(pt); k0 = mul24(pt & 15, QP); PIN(k0); }
    if constexpr (k == 1) { k0 += aR + ((pt >> 1) & 24); PIN(k0); }
    if constexpr (k == 2) { qa = k0 + 8 * QP; k1 = LM(0x00FF00FF00FF00FFull) ? k0 + 16 * QP : qa; PIN(qa); PIN(k1); }
    if constexpr (k == 3) {
#pragma unroll
      for (int e = 0; e < 4; e++) { md[e] = lmask(e, 64); PINS(md[e]); }
      lm0 = LM(md[1]) ? 0 : 0xFFFF0000u; lm1 = LM(md[3]) ? 0 : 0xFFFF0000u; PIN(lm0); PIN(lm1);
    }
    if constexpr (k == 4) { lm0 = LM(md[0]) ? lm0 : ~0u; lm1 = LM(md[2]) ? lm1 : ~0u; PIN(lm0); PIN(lm1); }
    if constexpr (k == 5) {
#pragma unroll
      for (int e = 0; e < 4; e++) mk[e] = LM(aM[e]) ? 0.0f : -3.0e38f;
      PIN(mk);
    }
    if constexpr (k == 6) { v0 = sm + mul24((pt >> 2) & 15, QP); PIN(v0); }
    if constexpr (k == 7) { v0 += aR + ((pt << 3) & 24); PIN(v0); }
    if constexpr (k == 8) { v1 = v0 + (LM(0xFFFFFFFFull) ? 16 * QP : 8 * QP); PIN(v1); }
    if constexpr (k == 9) { e0 = LM(0x1111111111111111ull) ? v0 : sm + L_ONE - 320; e1 = LM(0x1111111111111111ull) ? v1 : sm + L_ONE - 320; PIN(e0); PIN(e1); }
    if constexpr (k == 10) { q0 = 0xFFFFull; PINS(q0); oa = (pt << 4) & 1008; oc = LM(q0) ? oa : OOB; PIN(oa); PIN(oc); }
    if constexpr (k == 11) { tg = TAG(); fa = aF + layer * 4 + ((pt & 7) << 7); PIN(fa); }
  };
  auto attn = [&](auto LY, auto NH_) {
    constexpr int layer = LY, NH = NH_;
    const int w = w0; (void)w;
    if (!layer || !PR0) sfor<NPR>([&](auto K) { if (!PSP || K < 3 || K == 11) prep(LY, K); });
    const u32 sx = aX;
    __builtin_amdgcn_s_setprio(APRIO);
    BAR();
    u32x4 Q[NH], K0[NH], K1[NH];
    sfor<NH>([&](auto J) { constexpr int p = J / 2, h = J % 2;
      Q[J] = lds2<9 * p + 5 * h, 9 * p + 4>(qa); K0[J] = lds2<18 + 9 * p + 5 * h, 22 + 9 * p>(k0); K1[J] = lds2<18 + 9 * p + 5 * h, 22 + 9 * p>(k1); });
    SB();
    STAMP(3 + layer * 11);
    u32 so = aO + layer * (2 * REG_B), sc = aC + layer * (2 * REG_B);
    PINS(so); PINS(sc);
    if (!layer && FLE == 1) { fl = __builtin_amdgcn_raw_buffer_load_b32(rws, fa, 0, 16); SB(); }
    if (!layer && PSP) prep(LY, std::integral_constant<int, 10>{});
    if (layer == 0) {
      const u32x4 sv = {SENT, SENT, SENT, SENT};
      ST128S(sv, oa, sx); ST128S(sv, oa + 1024, sx); ST128S(sv, oc, sx + 2048);
      asm volatile("s_nop 1" :: "v"(sv) : "memory");
    }
    if (!layer && PSP) sfor<7>([&](auto K) { prep(LY, std::integral_constant<int, 3 + (K + 2) % 7>{}); });
    f32x4 s0[NH], s1[NH];
    SB();
    sfor<NH>([&](auto J) { constexpr int h = J % 2;
      lgw<3 * (NH - 1 - J)>(Q[J], K0[J], K1[J]);
      if (h) { Q[J].z = 0; if (!LM(q0)) Q[J].w = 0; } else { Q[J].w = 0; if (!LM(q0)) Q[J].z = 0; }
      s0[J] = mfma(K0[J], Q[J], mk); s1[J] = mfma(K1[J], Q[J], mk); SB(); });
    if (!layer && FLE == 2) { fl = __builtin_amdgcn_raw_buffer_load_b32(rws, fa, 0, 16); SB(); }
    u32x4 vm[NH], ve[NH / 2];
#pragma unroll
    for (int j = 0; j < NH; j++) {
      if (!(j & 1)) ve[j >> 1] = tr2(e0 + (j >> 1) * 72 + 320, e1 + (j >> 1) * 72 + 320);
      vm[j] = tr2(v0 + (j >> 1) * 72 + (j & 1) * 40 + 288, v1 + (j >> 1) * 72 + (j & 1) * 40 + 288);
    }
    SB();
    float mx[NH];
#pragma unroll
    for (int j = 0; j < NH; j++) {
#pragma unroll
      for (int e = 0; e < 4; e++) s0[j][e] = LM(md[e]) ? s1[j][e] : s0[j][e];
      mx[j] = fmx(fmx(fmx(s0[j][0], s0[j][1]), s0[j][2]), s0[j][3]);
    }
    const float cc = 0.23570226039551584f * 1.4426950408889634f;
    if constexpr (NH == 4) {
      sw32(mx[0], mx[1]); sw32(mx[2], mx[3]);
      mx[0] = fmx(mx[0], mx[1]); mx[2] = fmx(mx[2], mx[3]);
      sw16(mx[0], mx[2]);
      mx[0] = fmx(mx[0], mx[2]) * -cc; mx[2] = mx[0];
      sw16(mx[0], mx[2]);
      mx[1] = mx[0]; mx[3] = mx[2];
      sw32(mx[0], mx[1]); sw32(mx[2], mx[3]);
    } else {
      sw32(mx[0], mx[1]);
      mx[0] = mx[1] = fmx(mx[0], mx[1]);
      sw16(mx[0], mx[1]);
      mx[0] = mx[1] = fmx(mx[0], mx[1]) * -cc;
      sw32(mx[0], mx[1]);
    }
    f32x4 o[NH], ox[NH];
    const f32x4 z = {0, 0, 0, 0};
    SB();
    if (layer || !FLE) fl = __builtin_amdgcn_raw_buffer_load_b32(rws, fa, 0, 16);
#pragma unroll
    for (int j = 0; j < NH; j++) {
#pragma unroll
      for (int e = 0; e < 4; e++) s0[j][e] = __builtin_amdgcn_exp2f(__builtin_fmaf(s0[j][e], cc, mx[j]));
      const u32 a = pk(s0[j][0], s0[j][1]), c = pk(s0[j][2], s0[j][3]), al = a & lm0, cl = c & lm1;
      const u32x4 pf = {al, cl, a ^ al, c ^ cl};
      ox[j] = mfma(ve[j >> 1], pf, z);
      o[j] = mfma(vm[j], pf, z);
    }
    u32 om[2 * NH], oe[NH];
#pragma unroll
    for (int j = 0; j < NH; j++) {
      const int h = j & 1;
      float t = ox[j][2 - 2 * h], den = ox[j][3 - 2 * h];
      sw16(t, den);
      const float inv = __builtin_amdgcn_rcpf(den);
      float u[6] = {o[j][0] * inv, o[j][1] * inv, o[j][2] * inv, o[j][3] * inv, ox[j][2 * h] * inv, ox[j][2 * h + 1] * inv};
      if (!(layer && APK)) {
#pragma unroll
        for (int e = 0; e < 6; e++) asm("" : "+v"(u[e]));
      }
      om[2 * j] = pk(u[0], u[1]); om[2 * j + 1] = pk(u[2], u[3]); oe[j] = pk(u[4], u[5]);
      if (!h) { om[2 * j] = nos(om[2 * j]); oe[j] = nos(oe[j]); }
    }
    ST128S((u32x4{om[0], om[1], om[2], om[3]}), oa, so);
    if constexpr (NH == 4) {
      ST128S((u32x4{om[4], om[5], om[6], om[7]}), oa + 1024, so);
      ST128S((u32x4{oe[0], oe[1], oe[2], oe[3]}), oc, sc);
    } else __builtin_amdgcn_raw_buffer_store_b64(u32x2{oe[0], oe[1]}, rws, oc, sc, 16);
    STAMP(4 + layer * 11);
    SB();
  };
  auto puby = [&](u32x2* p, u32 a, u32 tag) {
    u32 e[2];
#pragma unroll
    for (int j = 0; j < 2; j++) {
      auto x = __builtin_amdgcn_permlane16_swap(p[2 * j].x, p[2 * j + 1].x, false, false), y = __builtin_amdgcn_permlane16_swap(p[2 * j].y, p[2 * j + 1].y, false, false);
      ST128C((u32x4{x[0], y[0], x[1], tag}), rws, a + j * 1024); e[j] = y[1];
    }
    ST128C((u32x4{e[0], e[1], tag, tag}), rws, a + 2048);
  };
  AS3 char* cvb[2]; AS3 char* cva[2];
  auto cadr = [&](int k) {
    IDS();
    const u32 ho = k ? L_H0 + (cw >> 1) * H_B + (cw & 1) * 256 : L_H2 + (r & 3) * 128, ko = k ? L_CONV + (cw >> 1) * KV_B + (cw & 1) * 256 : L_CONV + 2 * KV_B + (r & 3) * 128;
    cvb[k] = sm + ko + (240 - 16 * i + 8 * q) * 2; cva[k] = sm + ho + (i >> 3 ? CH_E + 4 : CH_O) + ((i & 7) + 4 * q) * 4;
    PIN(cvb[k]); PIN(cva[k]);
  };
  auto conv = [&](auto NR, f32x4* ac, auto hook) {
    constexpr int nr_ = NR;
    auto rd2 = [&](AS3 char* p) { return u32x2{*(AS3 u32*)p, *(AS3 u32*)(p + 4)}; };
    auto cat = [&](u32x2 a, u32x2 c) { return u32x4{a.x, a.y, c.x, c.y}; };
    AS3 char* ap[4];
    u32x2 Al[41], Ah[41];
    u32x4 B[40];
    AS3 char* bp = cvb[nr_ / 4];
    auto ldb = [&](int n) { B[n] = *(AS3 u32x4*)(bp + (7 - (n & 7)) * 512 + (n >> 3) * 64); };
    ap[0] = cva[nr_ / 4];
    hook(std::integral_constant<int, -1>{});
    Al[0] = rd2(ap[0]); Ah[0] = rd2(ap[0] + 8);
#pragma unroll
    for (int n = 0; n < 8; n++) ldb((n + 4) & 7);
#pragma unroll
    for (int k = 1; k < 4; k++) { ap[k] = ap[0] + k * 1024; PIN(ap[k]); }
    Al[7] = rd2(ap[3] + 512); Ah[7] = rd2(ap[3] + 520);
    sfor<36 * nr_>([&](auto N) {
      constexpr int rr = N / 36, G = N % 36 / 9, st = N % 9, L = 8 - G;
      constexpr int mm = st < L ? G : 7 - G, j = G == 0 && st < L ? (st + 4) & 7 : st < L ? st : st - L;
      SB();
      if (CVA && N >= 8 && N < 36 * nr_ - 36) mfa(ac[j + mm], cat(Al[rr * 8 + mm], Ah[rr * 8 + mm]), B[rr * 8 + j]);
      else ac[j + mm] = mfma(cat(Al[rr * 8 + mm], Ah[rr * 8 + mm]), B[rr * 8 + j], N < 8 ? f32x4{0, 0, 0, 0} : ac[j + mm]);
      if constexpr (rr < nr_ - 1 && (mm == 4 || (st < L && j + mm == 7))) ldb(rr * 8 + 8 + j);
      constexpr int S2 = L < 7 ? L : 7, nr = G == 3 ? rr + 1 : rr, nG = (G + 1) & 3;
      constexpr int F = nr * 8 + (st < 2 ? nG : 7 - nG);
      if constexpr (nr < nr_ && (st < 2 || st == S2 || st == S2 + 1)) {
        AS3 char* p = ap[(F & 7) >> 1] + (F & 1) * 512 + (F >> 3) * 64;
        if constexpr (st == 1 || st == S2 + 1) Ah[F] = rd2(p + 8); else Al[F] = rd2(p);
      }
      hook(N);
    });
    SB();
#pragma unroll
    for (int j = 0; j < 8; j++) PIN(ac[j]);
  };
  {
    IDS();
    ENTRY2();
    STAMP4(12);
    AS3 char* wp[2] = {sm + (8 * q + r4) * 432 + tcol(w, 0) + c4 * 8, sm + (8 * q + r4) * 432 + tcol(w, 1) + c4 * 8};
    AS3 char* wq[2];
    auto wpk = [&]() {
      wq[0] = sm + w * 1024 + lane * 16;
      wq[1] = sm + (w == 4 ? (i < 8 ? 12416 + lane * 16 : 13184 + q * 128 + i * 16) : (w < 4 ? 9 + w : w == 5 ? 7 : 8) * 1024 + lane * 16);
      PIN(wq[0]); PIN(wq[1]);
    };
    const u32 xe = i * 128 + ((q ^ (i & 7)) << 4), xo = xe ^ 64;
    const rsrc_t rwo = __builtin_amdgcn_make_buffer_rsrc((void*)pwo, 0, 576 * 1152, 0x20000);
    if (__builtin_expect(w == 7, 1)) {
      u32 vw[9], l16 = lane * 16;
      const u32 sp = RFL(WS_WP + m * 110592);
      const u32 vx = mul24(lane >> 3, 2304u) + (((lane & 7) ^ (lane >> 3)) << 4);
      const u32 sx = g * 73728, sh = g > 0 ? g * 73728 - 18432 : 0;
      const u32 so = RFL(WS_WO + m * 82944);
      const rsrc_t rh = __builtin_amdgcn_make_buffer_rsrc((void*)ph, 0, 576 * 4096, 0x20000);
      STAMP4(14);
      u32 pf[4], tag = 0, tp, sg, ea, sa, ca, hs[2], ks[2], cb;
      u32x4 sv;
      auto poll = [&]() { sfor<4>([&](auto J) { pf[J] = hl32<J * 1024>(rws, l16, WS_FLAG); }); };
      auto rdw = [&](auto S, int n) { AS3 char* a = (S >= PK0 ? wq : wp)[n & 1] + SLOT(S) * SLOT_B + n / 2 * 13824; (n & 1 ? wf1 : wf0)[2 * S + n / 2] = S >= PK0 ? *(AS3 u32x4*)a : tr2(a, a + 1728); SB(); };
      auto kvd = [&](auto C) { constexpr int c = C; dma4k<L_CONV + c * KV_B, 1>(rws, l16, ks[c / 2] + (c & 1) * 4096); };
      auto clr = [&](auto N) { constexpr int n = N;
        __builtin_amdgcn_raw_buffer_store_b128(sv, rws, n < 8 ? l16 + (n & 1) * 1024 : ca, cb + (n < 8 ? (n >> 1) * 18432 : 0), 16); };
      auto ev = [&](auto I) {
        constexpr int e = SQ.e[I];
        if constexpr (e < 333) {
          constexpr int s = e / 37, n = e % 37, c = n - 10, t = n >> 1;
          if constexpr (n >= 10 && s >= PK0) { if constexpr ((c & 3) && runs(I)) dmak<c & 3>(rws, l16, sg, 0); else dmaf<SLOT(s) * SLOT_B + c / 4 * 4096, c & 3, (s - 5) * 27648 + c / 4 * 4096>(rws, l16, sp, sg); }
          else if constexpr (n >= 10) dmai<SLOT(s) * SLOT_B + (c / 3 + (c % 3) * 9) * 1024, 0>(rw, vw[c / 3], s * 221184 + (c % 3) * 73728);
          else if constexpr (n == 8) dmae<SLOT(s) * SLOT_B + WSL_B + n * 1024, s * 128>(rx, vx, sh);
          else dmai<SLOT(s) * SLOT_B + WSL_B + n * 1024, s * 128>(rx, vx, t < 4 ? sx + ((t >> 1) * 32 + (n & 1) * 16 + (t & 1)) * 1152 : sh + (n & 1) * 1152);
        }
        if constexpr (e == E_KB) { STAMP3(12); STAMP4(15); wpk(); VMWN(since(-1, I)); STAMP4(16); BAR(); STAMP4(17); }
        if constexpr (e == E_FLAG) ST32C(tag, rws, lane == 0 ? WS_FLAG + b * 16 : OOB);
        if constexpr (e >= E_VW && e < E_CLR) { vw[e - E_VW] = seg9((e - E_VW) * 64 + lane) + m * 144; PIN(vw[e - E_VW]); }
        if constexpr (e == E_PREP) {
          PIN(l16);
          ea = (b | lane) == 0 ? WS_E : OOB; sa = lane < 32 ? g * 512 + l16 : lane < 40 ? gp * 512 + 320 + (lane & 4) * 32 + (lane & 3) * 16 : OOB;
          cb = RFL(gb + m * 2048); ca = q * 18432 + i * 16 + 16384 - m * 1792;
          hs[0] = RFL(ch0 * 4096); hs[1] = RFL(ch2 * 4096); ks[0] = hs[0] + WS_KVT; ks[1] = hs[1] + WS_KVT;
          sv = u32x4{SENT, SENT, SENT, SENT};
          tag = *(AS3 u32*)(sm + L_TAG); tp = *(AS3 u32*)(sm + L_MISC + (lane < 2 ? lane : 2) * 4);
          PIN(ea); PIN(sa); PIN(ca); PINS(cb); PINS(hs[0]); PINS(hs[1]); PINS(ks[0]); PINS(ks[1]); PIN(sv);
        }
        if constexpr (e == E_PPOLL) pf[0] = hld32c(rws, WS_PF + m * 512 + (l16 & 496));
        if constexpr (e == E_PCHK) {
          VMWN(since(E_PPOLL, I));
#pragma unroll 1
          for (int spin = 0; spin < SPIN_MAX; spin++) {
            PIN(pf[0]);
            if (__builtin_expect(!ANY(pf[0] != tag), 1)) break;
            pf[0] = hld32c(rws, WS_PF + m * 512 + (l16 & 496));
            VMW(0);
          }
        }
        if constexpr (e == E_UPOLL) poll();
        if constexpr (e == E_UCHK) {
          VMWN(since(E_UPOLL, I));
          auto miss = [&]() {
#pragma unroll
            for (int j = 0; j < 4; j++) PIN(pf[j]);
            return ANY((((pf[0] ^ tag) | (pf[1] ^ tag)) | ((pf[2] ^ tag) | (pf[3] ^ tag))) != 0);
          };
          if (__builtin_expect(miss(), 0)) {
#pragma unroll 1
            for (int spin = 0; spin < SPIN_MAX; spin++) { poll(); VMW(0); if (!miss()) break; }
          }
          ST32C(tag, rws, ea);
          dmaic<L_STAT, 0>(rws, sa, WS_XS);
        }
        if constexpr (e >= E_H && e < E_KV) { constexpr int c = e - E_H; dma4k<(c < 2 ? L_H0 + c * H_B : L_H2) + (c < 3 ? CH_E : CH_O) + 32, 0>(rh, l16, hs[c / 2] + (c == 1 ? 4096 : c == 3 ? 2 : 0)); }
        if constexpr (e >= E_KV && e < E_BAR) kvd(std::integral_constant<int, e - E_KV>{});
        if constexpr (e >= E_CLR && e < E_END) clr(std::integral_constant<int, e - E_CLR>{});
        if constexpr (e == E_END + 3) {
          if (lane < 34) {
#pragma unroll
            for (int c = 0; c < 3; c++) *(AS3 u32x4*)(sm + L_CONV + c * KV_B + 4096 + lane * 16) = u32x4{0, 0, 0, 0};
          }
        }
        if constexpr (e >= E_BAR && e < E_VW) { constexpr int s = e - E_BAR;
          if (s >= SO && s < SO + 3) STAMP3(13 + 2 * (s - SO));
          VMWN(c63(since(37 * s + 36, I)));
          if (s >= SO && s < SO + 3) STAMP3(14 + 2 * (s - SO));
          BAR();
        }
        if constexpr (e >= E_RDW) rdw(std::integral_constant<int, (e - E_RDW) / 4>{}, (e - E_RDW) % 4);
      };
      constexpr int I0 = sq_at(CS0), NS = (SQ.n - I0 + EVS - 1) / EVS;
      static_assert(CS0 > U_CHK && CS0 > BS[6] && NS < 60, "schedule");
      sfor<I0>(ev);
      {
        if (lane < 3) *(volatile AS3 unsigned short*)(sm + (lane < 2 ? L_H0 + lane * H_B : L_H2) + CH_E + 32) = tp;
        auto tl = [&](auto J) { constexpr int j = J, d = j < WN0 ? j - TK : j - TK - TC;
          if constexpr (j < TK) { if constexpr (j & 3) dmak<j & 3>(rws, l16, sg, 0); else dmaf<L_CONV + j / 4 * KV_B, 0, j * 1024>(rws, l16, ks[0], sg); }
          else if constexpr (j >= WN0 && j < WN0 + TC) clr(std::integral_constant<int, j - WN0>{});
          else if constexpr (d & 3) dmak<d & 3>(rws, l16, sg, 0);
          else dmaf<L_WO + (81 - WOT + d) * 1024, 0, (81 - WOT + d) * 1024>(rws, l16, so, sg);
        };
        STAMP2(15);
        f32x4 ac[9];
        cadr(0);
        auto tail = [&](auto N) {
          constexpr int n = N, nw = wo_n(n), n0 = n < 0 ? 0 : wo_n(n - 1);
          if constexpr (n < 0 && N29 < 4) sfor<4>([&](auto J) { rdw(std::integral_constant<int, 7>{}, J); });
          sfor<nw - n0>([&](auto J) { tl(std::integral_constant<int, n0 + J>{}); if constexpr (n0 + J + 1 == -N29) { VMWN(c63(since(332) - N29)); STAMP2(16); BAR(); } });
          if constexpr (n >= 0 && n < 4 && N29 >= 4) rdw(std::integral_constant<int, 7>{}, n);
          if constexpr (n == N29 && n >= 0) { VMWN(c63(since(332) + nw)); STAMP2(16); BAR(); }
          if constexpr (n >= NR8 && n < NR8 + 4) rdw(std::integral_constant<int, 8>{}, n - NR8);
          if constexpr (n == NFL) { VMWN(nw - WN0 - TC); PIN(sv); ST32C(tag, rws, lane == 0 ? WS_FLAG + b * 16 + 4 : OOB); }
        };
        conv(std::integral_constant<int, 2>{}, ac, [&](auto N) {
          constexpr int n = N;
          if constexpr (n >= 0 && n < NS) sfor<EVS>([&](auto K) { if constexpr (I0 + n * EVS + K < SQ.n) ev(std::integral_constant<int, I0 + n * EVS + K>{}); });
          if constexpr (n >= NS - 1) tail(std::integral_constant<int, n - NS>{});
          if constexpr (n < 0) { VMWN(c63(since(E_KV + 2, I0) + (NS ? 0 : wo_n(-1)))); STAMP2(18); }
        });
        constexpr int NP = PBY > 72 - NS ? PBY : 72 - NS;
        sfor<NP + NS - 72>([&](auto K) { tail(std::integral_constant<int, 72 - NS + K>{}); });
        static_assert(wo_n(71) == TK + TC + WOT && wo_n(NFL) >= WN0 + TC && NFL > N29 && NFL < 72 && -N29 <= wo_n(-1) && N29 < NR8 && NR8 < 68 && N29 < NP, "schedule");
        *(volatile AS3 u32*)(sm + L_SYNC + 68) = 1;
        const u32 yb = WS_Y + xs * YM_B + (64 + r) * YC_B + (i >> 2) * 128 + (q >> 1) * 64 + (i & 3) * 16 + (q & 1) * 512;
        u32x2 p[8];
#pragma unroll
        for (int j = 0; j < 8; j++) p[j] = u32x2{pk(ac[j][0], ac[j][1]), pk(ac[j][2], ac[j][3])};
        puby(p, yb, tag); puby(p + 4, yb + 3072, tag);
        SB(); asm volatile("s_nop 1" ::: "memory"); SB();
        sfor<72 - NP>([&](auto K) { tail(std::integral_constant<int, NP + K>{}); });
        if (NB9 >= 72) { STAMP2(19); BAR(); }
      }
      __builtin_amdgcn_s_setprio(0);
    } else {
      const rsrc_t rsk = __builtin_amdgcn_make_buffer_rsrc((void*)psk, 0, 576 * 8, 0x20000);
      STAMP4(13);
      u32x2 skv[2];
      u32 ev = 0, h0v = 0, dv = 0;
      if (w == 5) {
#pragma unroll
        for (int j = 0; j < 4; j++) dma16(rw, j * 64 + lane < 216 ? seg9(j * 64 + lane) : OOB, RFL((320 + 8 * g) * 3456 + m * 144), L_PS + j * 1024);
      }
#pragma unroll
      for (int j = 0; j < 2; j++) skv[j] = hld64(rsk, (j == 1 && w) ? 4096 + lane * 8 : xs * 512 + (j * 56 + (tid >> 3)) * 8);
      u32x4 xr[9];
      if (w == 3) {
#pragma unroll
        for (int j = 0; j < 9; j++) xr[j] = hld128(rx, g * 73728 + m * 9216 + mul24(lane >> 3, 1152u) + (lane & 7) * 16 + j * 128);
      }
      if (w == 4) dma4(rw, lane < 34 ? lane * 128 : OOB, b * 4352, L_MISC + 3072);
      if (w == 5) ev = hld32c(rws, WS_E);
      if (w == 2) {
#pragma unroll
        for (int j = 0; j < 3; j++) {
          const u32 e = j * 64 + lane, rl = mul24(e, 7282u) >> 16, hq = g >> 4;
          dma16(rwo, e < 162 ? mul24(rl + (rl < 8 ? 8 * hq : rl < 16 ? 12 + 8 * hq : 2 * hq), 1152u) + (e - rl * 9) * 16 : OOB, RFL((g & 15) * 41472 + m * 144), L_WS + j * 1024);
        }
      }
      const rsrc_t rh = __builtin_amdgcn_make_buffer_rsrc((void*)ph, 0, 576 * 4096, 0x20000);
      if (w < 3) {
        const rsrc_t rd = __builtin_amdgcn_make_buffer_rsrc((void*)pD, 0, 576 * 2, 0x20000);
        const u32 c3 = w < 2 ? ch0 + w : ch2;
        h0v = hld16(rh, c3 * 4096); dv = hld16(rd, c3 * 2);
        if (HP0 == 1) dma4(rh, lane < 32 ? lane * 128 : OOB, RFL(c3 * 4096), L_MISC + 3072);
      }
      u32 kz[19], ka_, kv_, kd;
      auto kpre = [&](int j) {
        const bool ex = j == 1 && w;
        const u32 cl = j * 56 + (tid >> 3);
        ka_ = L_KV + 640 + (ex ? 9360 + lane * 2 : (lane & 7) * 1040 + cl * 2); kv_ = ka_ + (ex ? 9760 : 10800);
        kd = WS_KVT + 4080 - r * 128 + (ex ? 512 + lane : xs * 64 + cl) * 4096 - (ex ? xs : lane & 7) * 16;
        PIN(ka_); PIN(kv_); PIN(kd);
#pragma unroll
        for (int n = 0; n < 19; n++) { kz[n] = 0; PIN(kz[n]); }
      };
      auto kjob = [&](int j) {
        u32* z = kz;
        sfor<19>([&](auto N) { constexpr int n = N; ldhi<(n < 3 ? 128 * n : n < 11 ? 16 + 128 * n : 128 * (n - 11))>(kz[n], n < 11 ? ka_ : kv_); });
        PIN(skv[j]);
        const float t0 = bhi(skv[j].y), t1 = blo(skv[j].y), t2 = bhi(skv[j].x), t3 = blo(skv[j].x);
        float o0[8];
        asm volatile("s_waitcnt lgkmcnt(8)" : "+v"(z[0]), "+v"(z[1]), "+v"(z[2]), "+v"(z[3]), "+v"(z[4]), "+v"(z[5]), "+v"(z[6]), "+v"(z[7]), "+v"(z[8]), "+v"(z[9]), "+v"(z[10]));
#pragma unroll
        for (int e = 0; e < 8; e++) { o0[e] = __builtin_fmaf(U2F(z[e + 3]), t3, __builtin_fmaf(U2F(z[e + 2]), t2, __builtin_fmaf(U2F(z[e + 1]), t1, U2F(z[e]) * t0))); PIN(o0[e]); }
        asm volatile("s_waitcnt lgkmcnt(0)" : "+v"(z[11]), "+v"(z[12]), "+v"(z[13]), "+v"(z[14]), "+v"(z[15]), "+v"(z[16]), "+v"(z[17]), "+v"(z[18]));
#pragma unroll
        for (int e = 0; e < 8; e++) o0[e] *= U2F(z[11 + e]);
        ST128C((u32x4{pk(o0[7], o0[6]), pk(o0[5], o0[4]), pk(o0[3], o0[2]), pk(o0[1], o0[0])}), rws, kd);
      };
      auto pack = [&]() {
        AS3 char* a = sm + L_PS + r4 * 432 + c4 * 8 + q * 32;
        const u32 d = WS_WP + m * 110592 + (g >> 2) * 13824 + (g & 3) * 256 + q * 1024 + i * 16;
        u32x4 f[4];
#pragma unroll
        for (int j = 0; j < 4; j++) { AS3 char* p = a + (j < 2 ? 144 + 128 * j : j == 3 ? 96 : q ? -32 : 400); f[j] = tr2(p, p + 1728); }
#pragma unroll
        for (int j = 0; j < 4; j++) ST128C(f[j], rws, j < 3 || q == 0 ? d + j * 4096 : q == 1 && i < 8 ? d + 12288 - (g & 3) * 128 : OOB);
      };
      kpre(0); wpk();
      if (w == 6) *(AS3 u32x2*)(sm + (q == 3 ? L_ONE : q * SLOT_B + WSL_B + 8192) + i * 8) = u32x2{0x3F803F80u, 0x3F803F80u};
      STAMP4(15);
      if (w == 3) VMW(9); else if (w == 4 || w == 5) VMW(1); else VMW(0);
      if (!PKL && w == 5) pack();
      if (w < 3) {
        PIN(h0v); PIN(dv);
        h0v = pk(blo(h0v) + blo(dv), 0.0f);
        AS3 char* hb = sm + (w < 2 ? L_H0 + w * H_B : L_H2);
        if (lane < 25) *(AS3 u32*)(lane < 16 ? hb + (lane < 8 ? CH_O : CH_E - 32) + lane * 4 : lane < 24 ? sm + L_SYNC + w * 32 + (lane - 16) * 4 : sm + L_MISC + w * 4) =
            lane == 7 ? h0v << 16 : lane > 23 ? h0v : 0;
      }
      BAR();
      STAMP(1);
      if (HP0 == 2 && w == 6) { dma4(rh, lane * 128, RFL(ch0 * 4096), L_MISC + 3072); dma4(rh, lane < 8 ? lane * 128 : OOB, RFL(ch2 * 4096 + (r & 3) * 1024), L_MISC + 3072); }
      kjob(0);
      STAMP(31);
#pragma unroll
      for (int t = 0; t < 10; t++) acc[t] = f32x4{0, 0, 0, 0};
      u32x4 B[90];
      AS3 char* sp; AS3 char* cp; AS3 char* qp[2];
      u32 hq;
      auto mm = [&](int n) {
        if (n >= 5 && n < 80) { mfa(acc[n % 5], wf0[n / 5], B[n]); mfa(acc[5 + n % 5], wf1[n / 5], B[n]); }
        else { acc[n % 5] = mfma(wf0[n / 5], B[n], acc[n % 5]); acc[5 + n % 5] = mfma(wf1[n / 5], B[n], acc[5 + n % 5]); }
      };
      AS3 char* hp;
      u32x4 hv[2];
      u32 he[2];
      f32x2 st[5];
      auto hcr = [&]() {
        hp = sm + L_H0 + 32 + (tid0 & 255) * 16;
        PIN(hp);
#pragma unroll
        for (int p = 0; p < 2; p++) { hv[p] = *(AS3 u32x4*)(hp + p * H_B + CH_E); he[p] = *(AS3 u32*)(hp + p * H_B + CH_E + 16); }
        SB();
      };
      auto hcw = [&]() {
#pragma unroll
        for (int p = 0; p < 2; p++) { const u32x4 v = hv[p];
          *(AS3 u32x4*)(hp + p * H_B + CH_O) = u32x4{__builtin_amdgcn_alignbit(v.y, v.x, 16), __builtin_amdgcn_alignbit(v.z, v.y, 16), __builtin_amdgcn_alignbit(v.w, v.z, 16), __builtin_amdgcn_alignbit(he[p], v.w, 16)}; }
        SB();
      };
#pragma unroll
      for (int s = 0; s < 9; s++) {
        if (s >= SO && s < SO + 3) STAMP3(13 + 2 * (s - SO));
        if (s == FLAGS) VMW(0);
        if (s >= SO && s < SO + 3) STAMP3(14 + 2 * (s - SO));
        BAR();
        if (s == 0 || s == 8 || !DG5) STAMP(21 + s);
        if (s == 8) { if (w < 4) __builtin_amdgcn_s_setprio(TP0); else __builtin_amdgcn_s_setprio(TP4); }
        const u32 sl = SLOT(s) * SLOT_B;
        u32x2 wh[8];
        AS3 char* xa[2]; AS3 char* wa[2];
        auto rd = [&](int k) {
          const int h = k / 9, j = k % 9;
          if (k == 0 || k == 3) { wa[k / 3] = (s >= PK0 ? wq : wp)[k / 3] + sl; if (SLOT(s) == 2) PIN(wa[k / 3]); }
          if (s < 8 ? j == 2 : j == 8) { xa[h] = sm + sl + WSL_B + (h ? xo : xe); if (SLOT(s)) PIN(xa[h]); }
          if (s >= PK0 && j < 5 && j != 2) { if (j % 3 == 0) (j ? wf1 : wf0)[2 * s + h] = *(AS3 u32x4*)(wa[j / 3] + h * 13824); }
          else if (j == 0 || j == 1 || j == 3 || j == 4) {
            const int f = j / 3, u = j % 3;
            wh[h * 4 + f * 2 + u] = trd(wa[f] + h * 13824 + u * 1728);
            if (u) { u32x2 a = wh[h * 4 + f * 2], c = wh[h * 4 + f * 2 + 1]; (f ? wf1 : wf0)[2 * s + h] = u32x4{a.x, a.y, c.x, c.y}; }
          } else { const int t = j == 2 ? 0 : j - 4; B[10 * s + 5 * h + t] = *(AS3 u32x4*)(xa[h] + t * 2048); }
        };
        SB();
        constexpr int RO[18] = {0, 1, 8, 3, 4, 9, 10, 17, 12, 13, 2, 11, 5, 14, 6, 15, 7, 16};
#pragma unroll
        for (int n = 0; n < (s < 8 ? 10 : 6); n++) {
          if (10 * s + n >= 5) mm(s == 8 && n == 5 ? 84 : 10 * s + n - 5);
          SB();
#pragma unroll
          for (int k = RPS * n; k < RPS * n + RPS && k < 18; k++) rd(s < 8 ? k : RO[k]);
          SB();
          if (s == EPS && n == EPN) {
            sp = sm + L_STAT + i * 8; cp = sm + L_CS + w * 128 + q * 16; hq = (i >> 3) * (32 * QP);
            qp[0] = sm + L_QKV + i * QP + tcol(w, 0) + q * 8; qp[1] = sm + L_QKV + i * QP + tcol(w, 1) + q * 8;
            PIN(sp); PIN(cp); PIN(hq); PIN(qp[0]); PIN(qp[1]); SB();
          }
          if (s == HCS && n == HCN && w < 4) hcr();
          if (s == HCS && n == HCW && w < 4) hcw();
          if (s == 8 && n == STN) {
            st[4] = *(AS3 f32x2*)(sp + 512);
            SB();
#pragma unroll
            for (int t = 0; t < 4; t++) st[t] = *(AS3 f32x2*)(sp + t * 128);
            SB();
          }
          if (s == HPF && n == CLN && w == 3) {
            __builtin_amdgcn_s_setprio(CLPRIO);
            dma4(rh, lane * 128, RFL(ch0 * 4096), L_MISC + 3072); dma4(rh, lane < 8 ? lane * 128 : OOB, RFL(ch2 * 4096 + (r & 3) * 1024), L_MISC + 3072);
            __builtin_amdgcn_s_setprio(0);
          }
        }
        if (s == 0) STAMP3(12);
        if (s == 0 && w < 2) { kpre(1); kjob(1); }
        if (s == 0 && w == 2) {
          VMW(1);
          const u32 hq = g >> 4, e = lane - 32, x1 = e >= 9, c = e - 9 * x1;
          AS3 char* a = sm + L_WS + (4 * (q & 1) + r4) * 144;
          AS3 char* a0 = a + (q >> 1) * 32 + c4 * 8; AS3 char* a4 = a + 128 + (c4 & 1) * 8;
          u32x4 f0 = tr2(a0, a0 + 1152), f1 = tr2(a0 + 64, a0 + 1216), f4 = tr2(a4, a4 + 1152);
          u32x4 rw_ = *(AS3 u32x4*)(sm + L_WS + (e < 18 ? 2304 + x1 * 144 + (x1 ? c : c == 0 ? 8 : c - 1) * 16 : 0));
          const u32 ob = WS_WO + m * 82944 + (g & 15) * 5184, d = ob + (q >> 1) * 1024 + hq * 512 + (lane & 31) * 16;
          ST128C(f0, rws, d);
          ST128C(f1, rws, d + 2880);
          ST128C(lane < 32 ? f4 : rw_, rws, lane < 32 ? (i < 8 ? ob + 2048 + hq * 2880 + q * 128 + i * 16 : OOB) : e < 18 ? ob + 2304 + hq * 288 + e * 16 : OOB);
        }
        if (s == 0 && w == 5) { VMW(1); PIN(ev); ev = ev * 0x9E3779B1u + 0x7F4A7C15u; *(AS3 u32*)(sm + L_TAG) = ev; ST32C(ev, rws, lane ? OOB : WS_PF + m * 512 + g * 16); }
        if (s == 0 && w == 3) {
          VMW(1);
          float s1 = 0, s2 = 0, mu, rho;
#pragma unroll
          for (int j = 0; j < 9; j++) {
            PIN(xr[j]);
#pragma unroll
            for (int e = 0; e < 4; e++) {
              const u32 xe_ = xr[j][e];
              const bf16x2 v = __builtin_bit_cast(bf16x2, xe_);
              s1 = __builtin_amdgcn_fdot2_f32_bf16(v, __builtin_bit_cast(bf16x2, 0x3F803F80u), s1, false); s2 = __builtin_amdgcn_fdot2_f32_bf16(v, v, s2, false);
            }
          }
          sfor<3>([&](auto K) { constexpr int c = K == 0 ? 0xB1 : K == 1 ? 0x4E : 0x141;
            s1 += U2F(__builtin_amdgcn_update_dpp(0, F2U(s1), c, 15, 15, true)); s2 += U2F(__builtin_amdgcn_update_dpp(0, F2U(s2), c, 15, 15, true)); });
          ln_scalars(s1, s2, mu, rho);
          const u32 u = 8 * m + (lane >> 3);
          ST64C((u32x2{F2U(mu), F2U(rho)}), rws, (lane & 7) ? OOB : WS_XS + g * 512 + (u >> 5) * 256 + (u & 1) * 128 + (u & 30) * 4);
        }
      }
      {
        STAMP(2);
        f32x4 cs[2];
        float eu[4];
        u32x2 ep, eq[9];
        mm(89);
        sfor<107>([&](auto O) {
          constexpr int o = O, t = o < 19 ? 4 : (o - 19) / 22, f = o < 19 ? 0 : (o - 19) % 22 / 11, k = (o + 3) % 11;
          sfor<16>([&](auto M) { constexpr int j = M, n = 80 + j / 4 + j % 4 / 2 * 5, a = j / 4 + j % 2 * 5;
            if constexpr ((j < 3 ? 0 : 4 + TS * (j - 3)) == o) { SB();
              acc[a] = mfma((j & 1 ? wf1 : wf0)[n / 5], B[n], acc[a]);
              SB(); } });
          if constexpr (o == CSW || o == CSW + 3) { SB(); if (i == 0) *(AS3 f32x4*)(cp + (o - CSW) / 3 * 64) = cs[(o - CSW) / 3]; SB(); }
          if constexpr (HCS == 9 && o == HCO) { SB(); hcr(); }
          if constexpr (o == TPN) { SB(); __builtin_amdgcn_s_setprio(0); SB(); }
          if constexpr (o < 8) { float x = U2F(__builtin_amdgcn_mov_dpp(F2U(acc[4 + o / 4 * 5][o % 4]), 0x150, 15, 15, true)); PIN(x); cs[o / 4][o % 4] = x; }
          else if constexpr (k < 4) { eu[k] = __builtin_fmaf(-st[t].x, cs[f][k], acc[5 * f + t][k]); PIN(eu[k]); }
          else if constexpr (k < 8) { eu[k - 4] *= st[t].y; PIN(eu[k - 4]); }
          else if constexpr (k < 10) { u32 x = pk(eu[2 * k - 16], eu[2 * k - 15]); PIN(x); ep[k - 8] = x; }
          else if constexpr (o >= EPW) *(AS3 u32x2*)(qp[f] + (t < 4 ? qrow(t) : hq)) = ep;
          else eq[(o - 8) / 11] = ep;
          if constexpr (o >= EPW && (o - EPW) % EPD == 0 && (o - EPW) / EPD < (EPW - 8) / 11) { constexpr int z = (o - EPW) / EPD;
            *(AS3 u32x2*)(qp[z ? (z - 1) & 1 : 0] + (z ? qrow((z - 1) / 2) : hq)) = eq[z]; }
        });
        static_assert(EPW < 19 || EPW + ((EPW - 8) / 11 - 1) * EPD < 107, "schedule");
        SB();
        STAMP2(14);
        if (HCS == 9 && w < 4) hcw();
        if (w < 4) attn(std::integral_constant<int, 0>{}, std::integral_constant<int, 4>{}); else { epi(acc[9], cs[1], st[4], qp[1] + hq); if (CAN) cadr(1); BAR(); }
      }
      asm volatile(".Lfk_en2:");
    }
  }
  auto phase = [&](auto LY) {
    constexpr int layer = LY, OW = layer ? OW1 : OW0, AW = layer ? AW1 : AW0;
    if (w0 >= 4) { IDS(); STAMP(3 + layer * 11); }
    auto arrive = [&](int k, u32 n) { asm volatile("" ::: "memory"); *(volatile AS3 u32*)(sm + L_SYNC + k * 16 + (w0 & 3) * 4) = n; asm volatile("" ::: "memory"); };
    auto await = [&](int k, u32 n) {
#pragma unroll 1
      for (int spin = 0; spin < (SPIN_MAX << 4); spin++) {
        u32x4 v = *(volatile AS3 u32x4*)(sm + L_SYNC + k * 16);
        u32 a = v.x < v.y ? v.x : v.y, c = v.z < v.w ? v.z : v.w;
        if (RFL(a < c ? a : c) >= n) break;
        __builtin_amdgcn_s_sleep(1);
      }
      asm volatile("" ::: "memory");
    };
    auto lwait = [&](int o) {
#pragma unroll 1
      for (int spin = 0; spin < (SPIN_MAX << 4); spin++) { if (RFL(*(volatile AS3 u32*)(sm + L_SYNC + o))) break; __builtin_amdgcn_s_sleep(1); }
      asm volatile("" ::: "memory");
    };
    auto peek = [&](int k) { return *(volatile AS3 u32x4*)(sm + L_SYNC + k * 16); };
    auto chk = [&](u32x4 v, int k, u32 n) {
      u32 a = v.x < v.y ? v.x : v.y, c = v.z < v.w ? v.z : v.w;
      if (__builtin_expect(RFL(a < c ? a : c) < n, 0)) await(k, n);
      asm volatile("" ::: "memory");
    };
    if (w0 < 4) {
      u32x2 res[5];
      auto ldres = [&]() {
        IDS();
        const u32 row = g * 64 + (cw >> 1) * 32 + 2 * i + (cw & 1);
        const u32 vo = layer ? gb + REG_B + cw * 18432 + (9 * m + (q >> 1)) * 256 + i * 16 + (q & 1) * 8 : row * 1152 + m * 144 + q * 8;
        u32 v4 = vo - (q >> 1) * (layer ? 256 : 16);
        PIN(v4);
#pragma unroll
        for (int t = 0; t < 5; t++) res[t] = hl64(layer ? rws : rx, t < 4 ? vo : v4, t * (layer ? 512 : 32));
      };
      if (__builtin_expect(ANY(fl != tg), 0)) {
#pragma unroll 1
        for (int spin = 0; spin < SPIN_MAX; spin++) { asm volatile("" ::: "memory"); if (!ANY(__builtin_amdgcn_raw_buffer_load_b32(rws, fa, 0, 16) != tg)) break; }
      }
      f32x4 ac[9];
      u32 tag, sv;
      {
        u32 oa;
        AS3 char* pc0; AS3 char* pd0; AS3 char* pa[2]; AS3 char* pb[2];
        u32x4 of[18];
        auto ldo = [&](auto J) { constexpr int k = J < 2 ? 16 + J : J - 2; of[J] = hl128<(k & 3) * 1024>(rws, oa, (k >> 2) * 4096); };
        u32x4 A[90];
        u32 mx;
        auto lda = [&](int n_) {
          const int n = n_ / 5 * 5 + OPM[layer][n_ % 5], k = n < 10 ? 16 + n / 5 : (n - 10) / 5, t = n % 5;
          if (k < 16) A[n] = *(AS3 u32x4*)((t == 4 ? pb[k >> 3] : pa[k >> 3] + t * 1024 + (t > 1 ? 832 : 0)) + (k & 7) * 5184);
          else { AS3 char* a = (t == 4 ? pd0 : pc0 + t * 32) + (k - 16) * 41472; A[n] = tr2(a, a + 5184); }
        };
        auto step = [&](auto N) {
          constexpr int n = N, j = n / 5;
          SB();
          if constexpr (n >= 10 && n % 5 == 0) {
            if (VMM) { __builtin_amdgcn_s_waitcnt(vmimm((OW + j < 18 ? OW + j : 18) - j - 1)); if (n < 85) asm volatile("v_mfma_f32_16x16x32_bf16 %0, %2, %1, %0" : "+v"(ac[0]), "+v"(of[j]) : "v"(A[n]) : "memory"); else asm volatile("" : "+v"(of[j]) : "v"(A[n]) : "memory"); }
            else { VMWN((OW + j < 18 ? OW + j : 18) - j - 1); PIN(of[j]); }
          }
          if (VMM && n >= 10 && n < 85 && n % 5 == 0) ; else if (MFA && n < 85) mfa(ac[n % 5], A[n], of[j]); else ac[n % 5] = mfma(A[n], of[j], ac[n % 5]);
          if (n == (AW > 18 ? 48 - AW : 30)) { pa[1] = pa[0] + 41472; pb[1] = pb[0] + 41472; PIN(pa[1]); PIN(pb[1]); }
          if (n >= 10 && n % 5 == (n < 85)) { mx = mx > of[j].x ? mx : of[j].x; PIN(mx); }
          if (n + AW < 90) lda(n + AW);
          if (n == 83) tag = TAG();
          if (n == 87) { u64 q0 = 0xFFFFull; PINS(q0); sv = tid0; PIN(sv); sv = LM(q0) ? (sv & 15) << 4 : OOB; PIN(sv); }
          if constexpr (n % 5 == 4 && j + OW < 18) { SB(); ldo(std::integral_constant<int, j + OW>{}); }
        };
        TRY(SENT - 1)
          ldres();
          IDS();
          const u32 ws_ = ((r4 & 1) ? 0 : 16) + c4 * 8, wt_ = ((r4 & 1) ? 128 : 0) + (c4 & 1) * 8;
          const u32 f4 = (q < 2 ? 2048 : 4672) + q * 128 + (i & 7) * 16;
          oa = gb + layer * (2 * REG_B) + cw * 18432 + lane * 16;
          pc0 = sm + L_WO + (72 * q + r4 + 16) * 144 + ws_; pd0 = pc0 + wt_ - ws_;
          pa[0] = sm + L_WO + lane * 16; pb[0] = sm + L_WO + f4;
          PIN(oa); PIN(pc0); PIN(pd0); PIN(pa[0]); PIN(pb[0]);
          if (!layer) { __builtin_amdgcn_s_sleep(ODLY0); STAMP(30); sfor<OW>(ldo); }
          __builtin_amdgcn_s_setprio(OPRIO);
          if (!layer) await(0, 1);
#pragma unroll
          for (int n = 0; n < AW - 2; n++) lda(n);
          SB();
          if (layer) { __builtin_amdgcn_s_sleep(ODLY); STAMP(10); sfor<OW>(ldo); SB(); }
          VMWN(OW);
#pragma unroll
          for (int t = 0; t < 5; t++) PIN(res[t]);
#pragma unroll
          for (int t = 0; t < 5; t++) ac[t] = f32x4{blo(res[t].x), bhi(res[t].x), blo(res[t].y), bhi(res[t].y)};
#pragma unroll
          for (int t = 0; t < 5; t++) PIN(ac[t]);
          lda(AW - 2); lda(AW - 1);
          VMWN(OW - 2); PIN(of[0]); PIN(of[1]);
          if (__builtin_expect(ANY(has2(of[0], of[1])), 0)) {
            VMW(0);
#pragma unroll
            for (int k = 2; k < OW; k++) PIN(of[k]);
#pragma unroll 1
            for (int sp = 0; sp < SPIN_MAX; sp++) {
              sfor<2>(ldo); VMW(0); PIN(of[0]); PIN(of[1]);
              if (!ANY(has2(of[0], of[1]))) break;
            }
            sfor<OW - 2>([&](auto J) { ldo(std::integral_constant<int, J + 2>{}); });
          }
          STAMP(5 + layer * 11);
          mx = 0;
          sfor<87>(step);
          SB();
        AGAIN(mx)
        sfor<3>([&](auto N) { step(std::integral_constant<int, 87 + N>{}); });
        SB();
      }
      {
        auto rowst = [&](f32x4 h, u32 a) {
          f32x4 su = (ac[0] + ac[1]) + (ac[2] + ac[3]) + h, sq = ac[0] * ac[0];
#pragma unroll
          for (int t = 1; t < 4; t++) sq = ac[t] * ac[t] + sq;
          sq = h * ac[4] + sq;
          const f32x2 s2 = su.xy + su.zw, q2 = sq.xy + sq.zw;
          float sum = s2.x + s2.y, ssq = q2.x + q2.y;
          xsum2(sum, ssq);
          const u32x4 ms = {F2U(sum), F2U(ssq), tag, tag};
          ST128S(ms, sv, a);
          asm volatile("s_nop 1" :: "v"(ms) : "memory");
        };
        rowst(ac[4] * 0.5f, aS + layer * (32 * 8192));
        IDS();
        STAMP(6 + layer * 11);
        __builtin_amdgcn_s_setprio(0);
        u32 sa = i * 16 + q * 512;
        const u32 sb = RFL((aS & ~2047u) + layer * (32 * 8192));
        u32x4 ys;
        if (layer) ys = peek(3);
        f32x4 yf[5], ay[5];
#pragma unroll
        for (int t = 0; t < 5; t++) { u32 lo = pk(ac[t][0], ac[t][1]), hi = pk(ac[t][2], ac[t][3]); PIN(lo); PIN(hi); ac[t] = f32x4{blo(lo), bhi(lo), blo(hi), bhi(hi)}; PIN(ac[t]); }
        PIN(sa);
        AS3 char* yp = sm + L_Y + (4 * q + r4) * YP + (cw & 1) * 64 + (cw >> 1) * 32 + c4 * 8;
        auto rdy = [&]() {
          u32x2 y[5];
          if (layer || YW != 1) {
#pragma unroll
            for (int t = 0; t < 5; t++) y[t] = trd(yp + t * 16 * YP);
          } else
          TRYN(0, SPIN_MAX << 4)
            if (spin > 1) __builtin_amdgcn_s_sleep(1);
            ys = peek(3);
            asm volatile("" ::: "memory");
#pragma unroll
            for (int t = 0; t < 5; t++) y[t] = trd(yp + t * 16 * YP);
          AGAIN((ys.x & ys.y & ys.z & ys.w) ^ 1)
#pragma unroll
          for (int t = 0; t < 5; t++) { yf[t] = f32x4{blo(y[t].x), bhi(y[t].x), blo(y[t].y), bhi(y[t].y)}; ay[t] = ac[t] * yf[t]; PIN(yf[t]); PIN(ay[t]); }
        };
        const rsrc_t ro = __builtin_amdgcn_make_buffer_rsrc(layer ? pout : pws, 0, layer ? 2048 * 1152 : WS_USED, 0x20000);
        const u32 row = g * 64 + (cw >> 1) * 32 + 2 * i + (cw & 1), U = 2 * (q & 1) + (q >> 1);
        u32 da = layer ? row * 1152 + m * 144 + U * 16 : gb + REG_B + cw * 18432 + (9 * m + U) * 256 + i * 16, db = da + (layer ? 64 : 1024), dc = q ? OOB : da + (layer ? 128 : 2048);
        PIN(da); PIN(db); PIN(dc); PIN(yp);
        if (layer) { chk(ys, 3, 2); rdy(); }
        __builtin_amdgcn_s_sleep(layer ? SDLY : SDLY0);
        STAMP(8 + layer * 11);
        if (!layer && !YW) ys = peek(3);
        u32x4 a = LD128C(rws, sa, sb), c = LD128C(rws, sa + 256, sb);
        if (!layer && YW == 3) {
          TRYN(0, YPN)
            if (spin > 1) __builtin_amdgcn_s_sleep(YPS);
            ys = peek(3);
          AGAIN((ys.x & ys.y & ys.z & ys.w) ^ 1)
        }
        const bool early = layer || YW == 1 || YW == 2 || RFL(ys.x & ys.y & ys.z & ys.w);
        asm volatile("" ::: "memory");
        if (!layer && YW == 2) await(3, 1);
        if (!layer && __builtin_expect(early, 1)) rdy();
        SB();
        if (__builtin_expect(ANY(((a.z ^ tag) | (c.z ^ tag)) != 0), 0)) {
#pragma unroll 1
          for (int spin = 0; spin < SPIN_MAX; spin++) { asm volatile("" ::: "memory");
            if (SRS) __builtin_amdgcn_s_sleep(SRS);
            a = LD128C(rws, sa, sb); c = LD128C(rws, sa + 256, sb);
            if (!ANY(((a.z ^ tag) | (c.z ^ tag)) != 0)) break;
          }
        }
        float s1 = U2F(a.x) + U2F(c.x), s2 = U2F(a.y) + U2F(c.y), mu, rho;
        xsum2(s1, s2);
        ln_scalars(-s1, s2, mu, rho);
        if (__builtin_expect(!early, 0)) { PIN(mu); PIN(rho); await(3, 1); rdy(); }
        STAMP(7 + layer * 11);
        u32x2 xo2[6];
#pragma unroll
        for (int t = 0; t < 5; t++) {
          const f32x4 o = ac[t] = (yf[t] * mu + ay[t]) * rho + ac[t];
          xo2[t] = u32x2{pk(o[0], o[1]), pk(o[2], o[3])};
        }
        xo2[5] = xo2[4];
#pragma unroll
        for (int t = 0; t < 6; t += 2) {
          auto a = __builtin_amdgcn_permlane16_swap(xo2[t].x, xo2[t + 1].x, false, false);
          auto c = __builtin_amdgcn_permlane16_swap(xo2[t].y, xo2[t + 1].y, false, false);
          __builtin_amdgcn_raw_buffer_store_b128(u32x4{layer ? a[0] : nos(a[0]), c[0], a[1], c[1]}, ro, t == 0 ? da : t == 2 ? db : dc, 0, layer ? 0 : 16);
        }
        SB();
        STAMP(9 + layer * 11);
        if (layer == 0) { BAR(); if (q >= 2) ac[4] = f32x4{0, 0, 0, 0}; rowst(ac[4], aS + 64 * 8192); }
      }
    } else {
      if (layer == 0) {
        IDS();
        __builtin_amdgcn_s_setprio(CPRIO);
        const int rh = cw & 1;
        u32 sy, tag, yb;
        AS3 char* rp;
        f32x4 ac[9];
        const u32 wob = RFL(cw * ((80 - WOT) * 256)), wos = RFL(WS_WO + m * 82944) + wob;
        u32 l16 = lane * 16; PIN(l16);
        if (!CAN) cadr(1);
        conv(std::integral_constant<int, 4>{}, ac, [&](auto N) {
          constexpr int d = (N - NBC - 1) / NBS;
          if constexpr (CSL && N == -1) {
            sfor<21 - WOT / 4>([&](auto D) { dmak<D & 3>(rws, l16, wos + D / 4 * 4096, L_WO + wob + D / 4 * 4096); });
            __builtin_amdgcn_s_sleep(CSL);
          }
          if constexpr (!CSL && N > NBC && (N - NBC - 1) % NBS == 0 && d < 21 - WOT / 4) dmak<d & 3>(rws, l16, wos + d / 4 * 4096, L_WO + wob + d / 4 * 4096);
          if constexpr (N == 128) { IDS(); sy = *(volatile AS3 u32*)(sm + L_SYNC + 68); tag = TAG(); rp = sm + L_RED + lane * 16 + cw * 2048; PIN(rp);
            yb = WS_Y + xs * YM_B + (i >> 2) * 128 + (q >> 1) * 64 + (i & 3) * 16 + (2 * r + (cw >> 1)) * YC_B + (cw & 1) * 3072 + (q & 1) * 512; PIN(yb); }
          if constexpr (N == NWO) { VMW(0); STAMP2(17); arrive(0, 1); }
        });
        STAMP(5);
        if (__builtin_expect(!RFL(sy), 0)) lwait(68);
        auto fin = [&](auto K) {
          constexpr int k = K, o = 4 - k;
#pragma unroll
          for (int j = 0; j < 2; j++)
            *(AS3 u32x4*)(rp + j * 1024) = u32x4{pk(ac[o + 2 * j][0], ac[o + 2 * j][1]), pk(ac[o + 2 * j][2], ac[o + 2 * j][3]), pk(ac[o + 1 + 2 * j][0], ac[o + 1 + 2 * j][1]), pk(ac[o + 1 + 2 * j][2], ac[o + 1 + 2 * j][3])};
          arrive(2, 1);
          u32x4 s[2];
#pragma unroll 1
          for (int spin = 0; spin < (SPIN_MAX << 4); spin++) {
            u32 f = *(volatile AS3 u32*)(sm + L_SYNC + 32 + (cw ^ 1) * 4);
            s[0] = *(volatile AS3 u32x4*)(rp + (k ? -2048 : 2048)); s[1] = *(volatile AS3 u32x4*)(rp + (k ? -1024 : 3072));
            if (RFL(f)) break;
          }
          u32x2 p[4];
#pragma unroll
          for (int j = 0; j < 2; j++) {
            f32x4 u = ac[k + 2 * j], v = ac[k + 2 * j + 1];
            p[2 * j] = u32x2{pk(u[0] + blo(s[j].x), u[1] + bhi(s[j].x)), pk(u[2] + blo(s[j].y), u[3] + bhi(s[j].y))};
            p[2 * j + 1] = u32x2{pk(v[0] + blo(s[j].z), v[1] + bhi(s[j].z)), pk(v[2] + blo(s[j].w), v[3] + bhi(s[j].w))};
          }
          puby(p, yb, tag);
        };
        if (rh) fin(std::integral_constant<int, 4>{}); else fin(std::integral_constant<int, 0>{});
        STAMP(6);
        __builtin_amdgcn_s_setprio(0);
      }
      IDS();
      if (layer && A8) __builtin_amdgcn_s_setprio(0);
      const u32 yo = WS_Y + m * YM_B + (g >> 4) * 3072 + (g & 7) * 128 + (lane & 7) * 16 + cw * 8 * YC_B, tag = TAG();
      u32 ya[2] = {yo + mul24(lane >> 3, YC_B), yo + mul24((i >> 3) * 4 + q, YC_B)};
      const u32 o0 = RFL((g & 8) * 128);
      PIN(ya[0]); PIN(ya[1]);
      const u32x4 cd = peek(2);
      if (layer == 0) { if (cw < 3) __builtin_amdgcn_s_sleep(YDLY); else __builtin_amdgcn_s_sleep(YDLY7); }
      if (layer == 0) STAMP2(12);
      u32x4 yv[3], ye[3];
      u32 d;
      d = 1;
      TRY(layer ? ~0u : 0)
        if (spin > 1) __builtin_amdgcn_s_sleep(YDL2);
        if (!YSEL || layer || d) {
          d = 0;
#pragma unroll
          for (int j = 0; j < 3; j++) { yv[j] = LD128C(rws, ya[j >> 1], o0 + j * 32 * YC_B); ye[j] = LD128C(rws, ya[j >> 1] + 2048, j * 32 * YC_B); }
#pragma unroll
          for (int j = 0; j < 3; j++) d |= (yv[j].w ^ tag) | (ye[j].w ^ tag);
        }
      AGAIN(d)
      if (layer == 0) STAMP2(13);
#pragma unroll
      for (int j = 0; j < 3; j++) yv[j].w = o0 ? ye[j].y : ye[j].x;
      if (layer == 0) chk(cd, 2, 1);
#pragma unroll
      for (int j = 0; j < 2; j++) *(AS3 u32x4*)(sm + L_Y + ((cw + 4 * j) * 8 + (lane >> 3)) * YP + (lane & 7) * 16) = yv[j];
      {
        auto a = __builtin_amdgcn_permlane32_swap(yv[2].x, yv[2].z, false, false), c = __builtin_amdgcn_permlane32_swap(yv[2].y, yv[2].w, false, false);
        auto e = __builtin_amdgcn_permlane16_swap(a[0], c[0], false, false), f = __builtin_amdgcn_permlane16_swap(a[1], c[1], false, false);
        *(AS3 u32*)(sm + L_Y + (64 + 2 * cw + (i >> 3)) * YP + (lane & 7) * 16 + q * 4) = pk((blo(e[0]) + blo(e[1])) + (blo(f[0]) + blo(f[1])), (bhi(e[0]) + bhi(e[1])) + (bhi(f[0]) + bhi(f[1])));
      }
      arrive(3, layer + 1);
      STAMP(7 + layer * 11);
      if (layer == 0) BAR();
    }
  };
  __builtin_amdgcn_s_waitcnt(0xC07F);
  phase(std::integral_constant<int, 0>{});
  {
    IDS();
    STAMP(11);
    AS3 char* xl[2] = {sm + lane * 16, sm + L_CONV + lane * 16};
    auto rdx = [&](int p, int t) { const u32 a = xk_addr(p, t); return *(AS3 u32x4*)(a < L_CONV ? xl[0] + a : xl[1] + (a - L_CONV)); };
    const f32x4 z = {0, 0, 0, 0};
    if (w == 7) {
      __builtin_amdgcn_s_setprio(W7Q);
      const u32 gpb = WS_SET + (gp >> 2) * SET_B + (gp & 3) * GRP_B, tag = TAG();
      u32 an = gb + REG_B + lane * 16, ah = gpb + REG_B + (2 + (i >> 3)) * 18432 + q * 256 + (8 + (i & 7)) * 16;
      u32 sn = WS_ST + (64 + g) * 8192 + q * 2048 + i * 16, sh = WS_ST + (64 + gp) * 8192 + (2 + (i >> 3)) * 2048 + q * 512 + (8 + (i & 7)) * 16;
      PIN(an); PIN(ah); PIN(sn); PIN(sh);
      u32 mx;
      auto see = [&](int p, int t) { const u32 v = rdx(p, t).x; mx = mx > v ? mx : v; };
      u32x4 sr[10];
      auto lds_ = [&](auto J) { constexpr int j = J; sr[j] = hl128<(j & 7) * 256>(rws, j < 8 ? sn : sh, 0); };
      if (XB) __builtin_amdgcn_s_sleep(XDLY2); else asm volatile("s_barrier" ::: "memory");
      STAMP(12);
      TRY(SENT - 1)
        sfor<44>([&](auto N) { constexpr int p = 7 + N / 4, t = N % 4; dmaic<xk_addr(p, t), (KS[p] & 3) * 1024>(rws, an, t * 18432 + (KS[p] >> 2) * 4096);
          if (N + 1 == XB && spin == 1) asm volatile("s_barrier" ::: "memory"); });
        STAMP(13);
        lds_(std::integral_constant<int, 8>{}); lds_(std::integral_constant<int, 9>{});
        mx = 0;
        sfor<11>([&](auto P) { VMWN(42 - 4 * P);
#pragma unroll
          for (int t = 0; t < 4; t++) see(7 + P, t); });
      AGAIN(mx)
      STAMP5(22);
      BAR();
      STAMP(15); STAMP5(23);
      f32x2 sth;
      VMW(0); PIN(sr[8]); PIN(sr[9]);
      if (__builtin_expect(ANY(((sr[8].z ^ tag) | (sr[9].z ^ tag)) != 0), 0)) {
        TRY(0)
          lds_(std::integral_constant<int, 8>{}); lds_(std::integral_constant<int, 9>{}); VMW(0); PIN(sr[8]); PIN(sr[9]);
        AGAIN((sr[8].z ^ tag) | (sr[9].z ^ tag))
      }
      TRY(SENT - 1)
        sfor<8>(lds_);
        sfor<18>([&](auto P) { constexpr int p = P; dmaic<xk_addr(p, 4), (KS[p] & 3) * 1024>(rws, ah, (KS[p] >> 2) * 4096); });
        VMW(18);
        u32 d = 0;
        float s1 = 0, s2 = 0, mu, rho;
#pragma unroll
        for (int j = 0; j < 8; j++) { PIN(sr[j]); d |= sr[j].z ^ tag; }
#pragma unroll
        for (int j = 0; j < 8; j++) { s1 += U2F(sr[j].x); s2 += U2F(sr[j].y); }
        ln_scalars(s1, s2, mu, rho);
        *(AS3 f32x2*)(sm + L_STAT + lane * 8) = f32x2{mu, rho};
        ln_scalars(xsum(U2F(sr[8].x) + U2F(sr[9].x)), xsum(U2F(sr[8].y) + U2F(sr[9].y)), mu, rho);
        sth = f32x2{mu, rho};
        *(AS3 f32x2*)(sm + L_STAT + 512 + i * 8) = sth;
        mx = d ? SENT : 0;
        sfor<3>([&](auto P) { VMWN(12 - 6 * P);
#pragma unroll
          for (int p = 0; p < 6; p++) see(6 * P + p, 4); });
      AGAIN(mx)
      u32x4 sv = {SENT, SENT, SENT, SENT};
      u32 l16 = lane * 16, ca = q * 18432 + i * 16 + 16384 - m * 1792, cb = RFL(gb + 2 * REG_B + m * 2048), fw = lane == 0 ? WS_FLAG + b * 16 + 8 : OOB;
      AS3 char* hw = sm + L_QKV + (i + (i >> 3) * 32) * QP + 368 + q * 8;
      f32x4 cs[2];
#pragma unroll
      for (int f = 0; f < 2; f++) { cs[f] = *(AS3 f32x4*)(sm + L_CS + (5 + f) * 128 + 64 + q * 16); }
      PIN(sv); PIN(l16); PIN(ca); PINS(cb); PIN(fw); PIN(hw); PIN(xl[1]);
      u32x4 B[18];
#pragma unroll
      for (int p = 0; p < 6; p++) B[p] = rdx(p, 4);
#pragma unroll
      for (int p = 0; p < 18; p++) {
        if (p == W7B) { __builtin_amdgcn_s_setprio(W7P); STAMP(16); BAR(); STAMP5(25); }
        SB();
        if (p && p < 16) mfa(acc[0], wf0[KS[p]], B[p]); else acc[0] = mfma(wf0[KS[p]], B[p], p ? acc[0] : z);
        SB();
        if (p + 6 < 18) B[p + 6] = rdx(p + 6, 4);
        SB();
        if (p && p < 16) mfa(acc[1], wf1[KS[p]], B[p]); else acc[1] = mfma(wf1[KS[p]], B[p], p ? acc[1] : z);
        SB();
        if (p < 9) __builtin_amdgcn_raw_buffer_store_b128(sv, rws, p < 8 ? l16 + (p & 1) * 1024 : ca, cb + (p < 8 ? (p >> 1) * 18432 : 0), 16);
      }
      SB();
      STAMP5(26);
#pragma unroll
      for (int f = 0; f < 2; f++) epi(acc[f], cs[f], sth, hw + 32 * f);
      if (PR0) sfor<NPR>([&](auto K) { prep(std::integral_constant<int, 1>{}, K); });
      SB();
      PIN(sv);
      VMW(0);
      STAMP5(27);
      ST32C(tag, rws, fw);
    } else {
      {
        u32 an = gb + REG_B + lane * 16 + (2 * w + 2 + (w > 3)) * 1024;
        AS3 char* xw = xl[1] + w * 4096;
        u32 xm = L_CONV + w * 4096;
        PIN(an); PIN(xw); PINS(xm);
        if (w < 4) __builtin_amdgcn_s_sleep(XDLY - XOF); else __builtin_amdgcn_s_sleep(XDLY);
        u32x4 v[4];
        u32 mx;
        TRY(SENT - 1)
          if (XRS && spin > 1) __builtin_amdgcn_s_sleep(XRS);
#pragma unroll
          for (int t = 0; t < 4; t++) { if (XDM) dma16c(rws, an, t * 18432, xm + t * 1024); else v[t] = LD128C(rws, an, t * 18432); }
          if (XDM) {
            VMW(0);
#pragma unroll
            for (int t = 0; t < 4; t++) v[t].x = *(AS3 u32*)(xw + t * 1024);
          }
          mx = mx3(v[0].x, v[1].x, v[2].x);
          mx = mx > v[3].x ? mx : v[3].x;
        AGAIN(mx)
#pragma unroll
        for (int t = 0; t < 4; t++) if (!XDM) *(AS3 u32x4*)(xw + t * 1024) = v[t];
      }
      AS3 char* sp; AS3 char* qp[2];
      f32x2 st[5];
      f32x4 cs[2];
      u32x4 B[90];
      BAR();
      STAMP(12);
#pragma unroll
      for (int n = 0; n < PD; n++) B[g2_x(n)] = rdx(g2_p(g2_x(n)), g2_t(g2_x(n)));
      u32 hq;
      float eu[4];
      u32x2 ep;
      auto fill = [&](auto M) {
        sfor<88>([&](auto O) { constexpr int e = O / 22, f = O % 22 / 11, k = O % 11;
          if constexpr (128 + 8 * e + EP0 + O % 22 * (e < 3 ? 8 : 10 - EP0) / 22 == M) {
            if constexpr (k < 4) { eu[k] = __builtin_fmaf(-st[e].x, cs[f][k], acc[5 * f + e][k]); PIN(eu[k]); }
            else if constexpr (k < 8) { eu[k - 4] *= st[e].y; PIN(eu[k - 4]); }
            else if constexpr (k < 10) { u32 x = pk(eu[2 * k - 16], eu[2 * k - 15]); PIN(x); ep[k - 8] = x; }
            else *(AS3 u32x2*)(qp[f] + qrow(e)) = ep;
          } });
      };
      sfor<90>([&](auto N) {
        constexpr int n = N, p = g2_p(n), t = g2_t(n);
        if (n + PD == 28 || n + PD == 64) { STAMP5(n < 40 ? 22 : 24); BAR(); STAMP5(n < 40 ? 23 : 25); }
        sfor<12>([&](auto J) { if (n == G2N[J]) __builtin_amdgcn_s_setprio(3 - J % 4); });
        SB();
        constexpr int ta = HC2 && t == 4 && (p & 1) ? 9 : t, p0 = HC2 && t == 4 ? p >> 1 : p;
        if (p0 && p < 16) mfa(acc[ta], wf0[KS[p]], B[n]); else acc[ta] = mfma(wf0[KS[p]], B[n], p0 ? acc[ta] : z);
        SB();
        if constexpr (g2_r(n) < g2_r(n + 1)) { constexpr int x = g2_x(g2_r(n)); B[x] = rdx(g2_p(x), g2_t(x)); }
        fill(std::integral_constant<int, g2_m(n)>{});
        if constexpr (t < 4) {
          SB();
          if (p && p < 16) mfa(acc[5 + t], wf1[KS[p]], B[n]); else acc[5 + t] = mfma(wf1[KS[p]], B[n], p ? acc[5 + t] : z);
          SB();
          fill(std::integral_constant<int, g2_m(n) + 1>{});
        }
        if constexpr (g2_r(n) + 1 < g2_r(n + 1)) { constexpr int x = g2_x(g2_r(n) + 1); B[x] = rdx(g2_p(x), g2_t(x)); }
        if (n == AD0) { IDS(); sp = sm + L_STAT + i * 8; PIN(sp); qp[0] = sm + L_QKV + i * QP + tcol(w, 0) + q * 8; qp[1] = sm + L_QKV + i * QP + tcol(w, 1) + q * 8; PIN(qp[0]); PIN(qp[1]); }
        if (n == AD0 + 2) { IDS(); cs[0] = *(AS3 f32x4*)(sm + L_CS + w * 128 + q * 16); cs[1] = *(AS3 f32x4*)(sm + L_CS + w * 128 + 64 + q * 16); }
        if constexpr (n >= 62 && (n - 62) % 6 == 0) st[(n - 62) / 6] = *(AS3 f32x2*)(sp + (n - 62) / 6 * 128);
        if constexpr (PR0 && n >= PR0 && n < PR0 + NPR * PRS && (n - PR0) % PRS == 0) prep(std::integral_constant<int, 1>{}, std::integral_constant<int, (n - PR0) / PRS>{});
        if (n == AD0 + 4) { IDS(); hq = (i >> 3) * (32 * QP); PIN(hq); }
      });
      SB();
      STAMP(13);
      epi(HC2 ? acc[4] + acc[9] : acc[4], cs[0], st[4], qp[0] + hq);
    }
    STAMP5(28);
    if (A8) attn(std::integral_constant<int, 1>{}, std::integral_constant<int, 2>{});
    else if (w0 < 4) attn(std::integral_constant<int, 1>{}, std::integral_constant<int, 4>{});
    else BAR();
  }
  phase(std::integral_constant<int, 1>{});
  asm volatile(".Lfk_end:");
}

extern "C" void kernel_launch(void* const* d_in, const int* in_sizes, int n_in, void* d_out, int out_size, void* d_ws, size_t ws_size,
                              hipStream_t stream) {
  fused_kernel<<<256, 512, 0, stream>>>(d_in[4], d_in[5], d_in[0], d_in[1], d_ws, d_in[6], d_in[7], d_in[2], d_in[8], d_out);
}
```
